```python
import torch

def _softmax(x, axis=-1):
    return torch.softmax(x, dim=axis)
def _gelu(x):
    return torch.nn.functional.gelu(x, approximate="tanh")
def _silu(x):
    return torch.nn.functional.silu(x)
def _sigmoid(x):
    return torch.sigmoid(x)
def _layernorm(x, axis=-1, eps=1e-5):
    m = x.mean(dim=axis, keepdim=True)
    v = x.var(dim=axis, keepdim=True, unbiased=False)
    return (x - m) / torch.sqrt(v + eps)
def _scatter_add(tgt, idx, src, axis=0):
    return tgt.clone().index_add_(axis, idx, src)
def _segment_sum(src, seg, n):
    out = torch.zeros((n,) + src.shape[1:], dtype=src.dtype)
    return out.index_add_(0, seg, src)
def _pad(x, pw):
    flat = tuple(v for pair in reversed(pw) for v in pair)
    return torch.nn.functional.pad(x, flat)

kernel_name = 'gated_mix__softmax_full_mha__mixer_token_mlp__prompt'

def setup_inputs():

    _w = (lambda a: (lambda t: t.to(torch.bfloat16) if t.is_floating_point() else t)(torch.as_tensor(a)))
    def _A_setup():
        B, S, D = 1, 2048, 512
        return {
            "x": _w((lambda *s: torch.randn(*s, dtype=torch.bfloat16) * 0.05)(B, S, D)),
            "wqkv": _w((lambda *s: torch.randn(*s, dtype=torch.bfloat16) * 0.8)(D, 3 * D)),
            "wo": _w((lambda *s: torch.randn(*s, dtype=torch.bfloat16) * 0.05)(D, D)),
        }
    _A_in = _A_setup()

    def _B_setup():
        B, S, D, F = 1, 2048, 512, 1376
        return {
            "x": _w((lambda *s: torch.randn(*s, dtype=torch.bfloat16) * 0.05)(B, S, D)),
            "w1": _w((lambda *s: torch.randn(*s, dtype=torch.bfloat16) * 0.8)(S, F) * (S ** -0.5)),
            "b1": _w((lambda *s: torch.randn(*s, dtype=torch.bfloat16) * 12.8)(F) * 0.02),
            "w2": _w((lambda *s: torch.randn(*s, dtype=torch.bfloat16) * 0.05)(F, S) * (F ** -0.5)),
            "b2": _w((lambda *s: torch.randn(*s, dtype=torch.bfloat16) * 0.8)(S) * 0.02),
        }
    _B_in = _B_setup()

    return {
        "A_x": _A_in["x"],
        "A_wqkv": _A_in["wqkv"],
        "A_wo": _A_in["wo"],
        "B_x": _B_in["x"],
        "B_w1": _B_in["w1"],
        "B_b1": _B_in["b1"],
        "B_w2": _B_in["w2"],
        "B_b2": _B_in["b2"],
        "gate_w": _w((lambda *s: torch.randn(*s, dtype=torch.bfloat16) * 0.05)(512) * 16.0),
    }

def reference(A_x, A_wqkv, A_wo, B_x, B_w1, B_b1, B_w2, B_b2, gate_w):
    def _A_ref(x, wqkv, wo):
        B, S, D = x.shape
        H = 32; Dh = D // H
        qkv = (lambda x, *d: x.permute(*d))((x @ wqkv).reshape(B, S, 3, H, Dh), 2, 0, 3, 1, 4)
        q, k, v = qkv[0], qkv[1], qkv[2]
        scale = Dh ** -0.5
        scores = torch.einsum("bhqd,bhkd->bhqk", q, k) * scale
        p = _softmax(scores, axis=-1)
        o = torch.einsum("bhqk,bhkd->bhqd", p, v)
        return (lambda x, *d: x.permute(*d))(o, 0, 2, 1, 3).reshape(B, S, D) @ wo

    def _B_ref(x, w1, b1, w2, b2):
        B, S, D = x.shape
        h = _layernorm(x, axis=-1)
        u = torch.einsum("bsd,sf->bdf", h, w1) + b1
        u = _gelu(u)
        y = torch.einsum("bdf,fs->bds", u, w2) + b2
        return (lambda x, *d: x.permute(*d))(y, 0, 2, 1)

    x = A_x
    g = _sigmoid((x * gate_w).sum(-1, keepdims=True))
    a = _A_ref(x, A_wqkv, A_wo)
    b = _B_ref(x, B_w1, B_b1, B_w2, B_b2)
    return g * a + (1 - g) * b
```

```cpp
#include <hip/hip_runtime.h>
#include <hip/hip_bf16.h>
typedef unsigned short u16;
__device__ __forceinline__ float bf2f(u16 v) { return __uint_as_float(((unsigned)v) << 16); }
__device__ __forceinline__ u16 f2bf(float f) { __bf16 b = (__bf16)f; return __builtin_bit_cast(u16, b); }
constexpr int S = 2048, D = 512, F = 1376, H = 32;

__global__ void n_qkv(const u16* x, const u16* w, float* qkv) {
  int n = blockIdx.x * 256 + threadIdx.x, s = blockIdx.y;
  float a = 0;
  for (int k = 0; k < D; k++) a += bf2f(x[s * D + k]) * bf2f(w[k * 1536 + n]);
  qkv[s * 1536 + n] = bf2f(f2bf(a));
}
__global__ void n_attn(const float* qkv, float* o) {
  int s = blockIdx.x * 64 + threadIdx.x, h = blockIdx.y;
  float q[16];
  for (int d = 0; d < 16; d++) q[d] = qkv[s * 1536 + h * 16 + d];
  float m = -1e30f;
  for (int j = 0; j < S; j++) { float a = 0; for (int d = 0; d < 16; d++) a += q[d] * qkv[j * 1536 + 512 + h * 16 + d]; m = fmaxf(m, a * 0.25f); }
  float l = 0, acc[16] = {0};
  for (int j = 0; j < S; j++) { float a = 0; for (int d = 0; d < 16; d++) a += q[d] * qkv[j * 1536 + 512 + h * 16 + d];
    float p = __expf(a * 0.25f - m); l += p; for (int d = 0; d < 16; d++) acc[d] += p * qkv[j * 1536 + 1024 + h * 16 + d]; }
  for (int d = 0; d < 16; d++) o[s * D + h * 16 + d] = bf2f(f2bf(acc[d] / l));
}
__global__ void n_ln(const u16* x, const u16* gw, float* hh, float* g) {
  int s = blockIdx.x * 64 + threadIdx.x;
  float m = 0, gg = 0; for (int d = 0; d < D; d++) { m += bf2f(x[s * D + d]); gg += bf2f(x[s * D + d]) * bf2f(gw[d]); } m /= D;
  float v = 0; for (int d = 0; d < D; d++) { float t = bf2f(x[s * D + d]) - m; v += t * t; } v /= D;
  float r = 1.0f / sqrtf(v + 1e-5f);
  for (int d = 0; d < D; d++) hh[s * D + d] = bf2f(f2bf((bf2f(x[s * D + d]) - m) * r));
  g[s] = 1.0f / (1.0f + expf(-gg));
}
__global__ void n_g1(const float* hh, const u16* w1, const u16* b1, float* u) {
  int f = blockIdx.x * 32 + (threadIdx.x & 31), d = blockIdx.y * 8 + (threadIdx.x >> 5);
  float a = 0; for (int s = 0; s < S; s++) a += hh[s * D + d] * bf2f(w1[s * F + f]);
  a += bf2f(b1[f]);
  float t = tanhf(0.7978845608028654f * (a + 0.044715f * a * a * a));
  u[d * F + f] = bf2f(f2bf(0.5f * a * (1.0f + t)));
}
__global__ void n_fin(const float* o, const u16* wo, const float* u, const u16* w2, const u16* b2, const float* g, u16* out) {
  int s = blockIdx.x * 64 + (threadIdx.x & 63), d = blockIdx.y * 4 + (threadIdx.x >> 6);
  float a = 0; for (int c = 0; c < D; c++) a += o[s * D + c] * bf2f(wo[c * D + d]);
  float y = 0; for (int f = 0; f < F; f++) y += u[d * F + f] * bf2f(w2[f * S + s]);
  y += bf2f(b2[s]);
  out[s * D + d] = f2bf(g[s] * a + (1.0f - g[s]) * y);
}
extern "C" void kernel_launch(void* const* d_in, const int* in_sizes, int n_in, void* d_out, int out_size, void* d_ws, size_t ws_size, hipStream_t stream) {
  const u16* x = (const u16*)d_in[0]; const u16* wqkv = (const u16*)d_in[1]; const u16* wo = (const u16*)d_in[2];
  const u16* w1 = (const u16*)d_in[4]; const u16* b1 = (const u16*)d_in[5]; const u16* w2 = (const u16*)d_in[6]; const u16* b2 = (const u16*)d_in[7]; const u16* gw = (const u16*)d_in[8];
  float* ws = (float*)d_ws;
  float* qkv = ws; float* o = qkv + S * 1536; float* hh = o + S * D; float* g = hh + S * D; float* u = g + S;
  n_qkv<<<dim3(6, S), 256, 0, stream>>>(x, wqkv, qkv);
  n_attn<<<dim3(S / 64, H), 64, 0, stream>>>(qkv, o);
  n_ln<<<S / 64, 64, 0, stream>>>(x, gw, hh, g);
  n_g1<<<dim3(F / 32, D / 8), 256, 0, stream>>>(hh, w1, b1, u);
  n_fin<<<dim3(S / 64, D / 4), 256, 0, stream>>>(o, wo, u, w2, b2, g, (u16*)d_out);
}
```

```cpp
#include <hip/hip_runtime.h>
#include <hip/hip_bf16.h>

typedef unsigned short u16;
typedef unsigned int u32;
typedef float f32x4 __attribute__((ext_vector_type(4)));
typedef float f32x16 __attribute__((ext_vector_type(16)));
typedef u32 u32x2 __attribute__((ext_vector_type(2)));
typedef u32 u32x4 __attribute__((ext_vector_type(4)));
typedef u32 u32x32 __attribute__((ext_vector_type(32)));
typedef __bf16 bf16x8 __attribute__((ext_vector_type(8)));
typedef __bf16 bf16x2 __attribute__((ext_vector_type(2)));
typedef short s16x4 __attribute__((ext_vector_type(4)));
#define LAS __attribute__((address_space(3)))
#define GAS __attribute__((address_space(1)))
#define DEV __device__ __forceinline__

constexpr int S_ = 2048, D_ = 512, F_ = 1376;
constexpr size_t WS_Q = 0, WS_K = 2u << 20, WS_V = 4u << 20, WS_H = 6u << 20, WS_O = 8u << 20, WS_U = 10u << 20, WS_P = 12u << 20,
                 WS_G = 24u << 20, WS_NK = WS_G + 8192, WS_NQ = WS_NK + 8192;
constexpr float QSCALE = 0.25f * 1.4426950408889634f;
constexpr float SAFE_BOUND2 = 50.0f * 50.0f;

DEV float bf_lo(u32 v) { return __uint_as_float(v << 16); }
DEV float bf_hi(u32 v) { return __uint_as_float(v & 0xffff0000u); }
DEV u32 pack2(float a, float b) { bf16x2 t = {(__bf16)a, (__bf16)b}; return __builtin_bit_cast(u32, t); }
DEV f32x16 mfma32(u32x4 a, u32x4 b, f32x16 c) {
  return __builtin_amdgcn_mfma_f32_32x32x16_bf16(__builtin_bit_cast(bf16x8, a), __builtin_bit_cast(bf16x8, b), c, 0, 0, 0);
}
DEV void glds16(const void* base, u32 voff, u32 ldsdst) {
  asm volatile("s_mov_b32 m0, %2\n\ts_nop 0\n\tglobal_load_lds_dwordx4 %0, %1" :: "v"(voff), "s"(base), "s"(ldsdst) : "memory");
}
#define VMWAIT(n) asm volatile("s_waitcnt vmcnt(" #n ")" ::: "memory")
#define LGKMWAIT(n) asm volatile("s_waitcnt lgkmcnt(" #n ")" ::: "memory")
DEV void raw_barrier() { asm volatile("s_waitcnt lgkmcnt(0)\n\ts_barrier" ::: "memory"); }
DEV u32x4 lds_r128(u32 a) { return *(const LAS u32x4*)(size_t)a; }
DEV u32x2 lds_tr(u32 a) { s16x4 t = __builtin_amdgcn_ds_read_tr16_b64_v4i16((LAS s16x4*)(size_t)a); return __builtin_bit_cast(u32x2, t); }
DEV u32x4 lds_tr2(u32 a0, u32 a1) { u32x2 x = lds_tr(a0), y = lds_tr(a1); u32x4 r = {x[0], x[1], y[0], y[1]}; return r; }
DEV __amdgpu_buffer_rsrc_t mkrsrc(void* base) { return __builtin_amdgcn_make_buffer_rsrc(base, 0, 0x02000000, 0x00020000); }
DEV void st16_wt(void* base, u32 voff, u32x4 v) { __builtin_amdgcn_raw_buffer_store_b128(v, mkrsrc(base), voff, 0, 16); }
DEV void st4_wt(void* base, u32 voff, u32 v) { __builtin_amdgcn_raw_buffer_store_b32(v, mkrsrc(base), voff, 0, 16); }
DEV void ld16_async(u32x4& d, const void* base, u32 voff) {
  asm volatile("global_load_dwordx4 %0, %1, %2 sc1" : "=v"(d) : "v"(voff), "s"(base) : "memory");
}
DEV void ld4_async(u32& d, const void* base, u32 voff) {
  asm volatile("global_load_dword %0, %1, %2 sc1" : "=v"(d) : "v"(voff), "s"(base) : "memory");
}
template <int CTRL> DEV float dppf(float v) { return __uint_as_float(__builtin_amdgcn_update_dpp(0, __float_as_int(v), CTRL, 0xf, 0xf, true)); }
DEV float row16_max(float m) {
  m = fmaxf(m, dppf<0xB1>(m)); m = fmaxf(m, dppf<0x4E>(m)); m = fmaxf(m, dppf<0x141>(m)); m = fmaxf(m, dppf<0x140>(m)); return m;
}
DEV float row16_sum(float m) {
  m += dppf<0xB1>(m); m += dppf<0x4E>(m); m += dppf<0x141>(m); m += dppf<0x140>(m); return m;
}
DEV float rdlane(float v, int l) { return __uint_as_float(__builtin_amdgcn_readlane(__float_as_int(v), l)); }
DEV float wave_sum(float m) { m = row16_sum(m); return (rdlane(m, 0) + rdlane(m, 16)) + (rdlane(m, 32) + rdlane(m, 48)); }
DEV float wave_max(float m) { m = row16_max(m); return fmaxf(fmaxf(rdlane(m, 0), rdlane(m, 16)), fmaxf(rdlane(m, 32), rdlane(m, 48))); }
DEV float xhalf(float v) {
  u32 a = __float_as_uint(v);
  auto r = __builtin_amdgcn_permlane32_swap(a, a, false, false);
  return __uint_as_float((threadIdx.x & 32) ? r[0] : r[1]);
}

struct Params {
  const u16 *x, *wqkv, *wo, *w1, *b1, *w2, *b2, *gw;
  u16* out; char* ws;
};

extern __shared__ __attribute__((aligned(16))) char smem[];

constexpr u32 P1_X = 0, P1_W = 65536, P1_WSLOT = 24576, P1_LN = P1_W + 3 * P1_WSLOT;
DEV void phase1(const Params& p, int tg, int g, int w, int lane) {
  u32x4 xr, gr;
  const int tok = tg * 64 + g * 8 + (w & 7);
  if (w < 8) {
    asm volatile("global_load_dwordx4 %0, %1, %2" : "=v"(xr) : "v"(tok * 1024 + lane * 16), "s"(p.x) : "memory");
    asm volatile("global_load_dwordx4 %0, %1, %2" : "=v"(gr) : "v"(lane * 16), "s"(p.gw) : "memory");
  }
  const int rot = tg & 7;
  u32 wv = 0;
  auto issue_w = [&](int it) {
    int kc = (it + rot) & 7; u32 slot = P1_W + (it % 3) * P1_WSLOT + (w - 8) * 2048;
    const char* b = (const char*)p.wqkv + kc * 64 * 3072;
    #pragma unroll
    for (int q = 0; q < 3; q++) { glds16(b + q * 1024, wv, slot + q * 8192); glds16(b + q * 1024 + 8 * 3072, wv, slot + q * 8192 + 1024); }
  };
  if (w < 8) {
    #pragma unroll
    for (int i = 0; i < 8; i++) { int r = w + 8 * i; glds16(p.x, (tg * 64 + r) * 1024 + ((lane ^ (r & 15)) << 4), P1_X + r * 1024); }
  } else {
    int r = lane >> 3, c = (lane & 7) ^ (((r >> 1) & 1) << 2);
    wv = ((w - 8) * 16 + r) * 3072 + g * 128 + c * 16;
    issue_w(0); issue_w(1); issue_w(2);
  }
  float gate = 0.f;
  if (w < 8) {
    asm volatile("s_waitcnt vmcnt(8)" : "+v"(xr), "+v"(gr) :: "memory");
    float v[8], s = 0.f, gs = 0.f;
    for (int i = 0; i < 4; i++) { v[2 * i] = bf_lo(xr[i]); v[2 * i + 1] = bf_hi(xr[i]); s += v[2 * i] + v[2 * i + 1]; gs += v[2 * i] * bf_lo(gr[i]) + v[2 * i + 1] * bf_hi(gr[i]); }
    float mean = wave_sum(s) * (1.0f / 512.0f); gs = wave_sum(gs);
    float q = 0.f;
    for (int i = 0; i < 8; i++) { v[i] -= mean; q += v[i] * v[i]; }
    float rstd = __builtin_amdgcn_rsqf(wave_sum(q) * (1.0f / 512.0f) + 1e-5f);
    u32x4 h = {pack2(v[0] * rstd, v[1] * rstd), pack2(v[2] * rstd, v[3] * rstd), pack2(v[4] * rstd, v[5] * rstd), pack2(v[6] * rstd, v[7] * rstd)};
    *(LAS u32x4*)(size_t)(P1_LN + w * 1024 + lane * 16) = h;
    gate = __builtin_amdgcn_rcpf(1.0f + __builtin_amdgcn_exp2f(-1.4426950408889634f * gs));
  }
  const int tt = w & 1, ct = w >> 1, part = ct >> 1, jc = ct & 1;
  const int c31 = lane & 31, h = lane >> 5;
  const u32 xrow = P1_X + (tt * 32 + c31) * 1024;
  u32 wa0, wa1;
  {
    int g4 = (lane >> 4) & 1, i = lane & 15, q = i >> 2;
    int r0 = 8 * h + q, r1 = r0 + 4, ch = 4 * jc + 2 * g4 + ((i & 3) >> 1);
    wa0 = part * 8192 + r0 * 128 + ((ch ^ (((r0 >> 1) & 1) << 2)) << 4) + (i & 1) * 8;
    wa1 = part * 8192 + r1 * 128 + ((ch ^ (((r1 >> 1) & 1) << 2)) << 4) + (i & 1) * 8;
  }
  f32x16 acc = {0};
  #pragma unroll
  for (int it = 0; it < 8; it++) {
    if (w >= 8) { if (it == 0) VMWAIT(12); else if (it < 7) VMWAIT(6); else VMWAIT(0); }
    else if (it == 0) VMWAIT(0);
    raw_barrier();
    if (w >= 8 && it >= 1 && it + 2 < 8) issue_w(it + 2);
    if (it == 0 && w < 8) {
      int d = w * 64 + lane; u32 t[8];
      for (int j = 0; j < 8; j++) t[j] = *(const LAS u16*)(size_t)(P1_LN + j * 1024 + d * 2);
      u32x4 o = {t[0] | (t[1] << 16), t[2] | (t[3] << 16), t[4] | (t[5] << 16), t[6] | (t[7] << 16)};
      st16_wt(p.ws + WS_H, (tg * 8 + g) * 8192 + d * 16, o);
      if (lane == 0) st4_wt(p.ws + WS_G, tok * 4, __float_as_uint(gate));
    }
    int kc = (it + rot) & 7; u32 slot = P1_W + (it % 3) * P1_WSLOT;
    #pragma unroll
    for (int k4 = 0; k4 < 4; k4++) {
      u32x4 xf = lds_r128(xrow + (((2 * (4 * kc + k4) + h) ^ (c31 & 15)) << 4));
      u32x4 wf = lds_tr2(slot + wa0 + k4 * 2048, slot + wa1 + k4 * 2048);
      acc = (part < 2) ? mfma32(wf, xf, acc) : mfma32(xf, wf, acc);
    }
  }
  const int tile = tg * 2 + tt;
  if (part < 2) {
    if (part == 0) for (int i = 0; i < 16; i++) acc[i] *= QSCALE;
    float n0 = 0.f, n1 = 0.f;
    for (int i = 0; i < 8; i++) { n0 += acc[i] * acc[i]; n1 += acc[8 + i] * acc[8 + i]; }
    n0 += xhalf(n0); n1 += xhalf(n1);
    n0 = row16_max(n0); n1 = row16_max(n1);
    n0 = fmaxf(rdlane(n0, 0), rdlane(n0, 16)); n1 = fmaxf(rdlane(n1, 0), rdlane(n1, 16));
    int head = 4 * g + 2 * jc;
    int slot = (part == 0) ? lane : ((lane & ~12) | ((lane & 4) << 1) | ((lane & 8) >> 1));
    char* dst = p.ws + (part == 0 ? WS_Q : WS_K);
    u32x4 a = {pack2(acc[0], acc[1]), pack2(acc[2], acc[3]), pack2(acc[4], acc[5]), pack2(acc[6], acc[7])};
    u32x4 b = {pack2(acc[8], acc[9]), pack2(acc[10], acc[11]), pack2(acc[12], acc[13]), pack2(acc[14], acc[15])};
    st16_wt(dst, ((head * 64 + tile) * 64 + slot) * 16, a);
    st16_wt(dst, (((head + 1) * 64 + tile) * 64 + slot) * 16, b);
    if (lane < 2) st4_wt(p.ws + (part == 0 ? WS_NQ : WS_NK), ((head + lane) * 64 + tile) * 4, __float_as_uint(lane ? n1 : n0));
  } else {
    int head = 4 * g + 2 * jc + (c31 >> 4);
    u32 o = ((head * 64 + tile) * 64 + h * 16 + (c31 & 15)) * 16;
    u32x4 a = {pack2(acc[0], acc[1]), pack2(acc[2], acc[3]), pack2(acc[4], acc[5]), pack2(acc[6], acc[7])};
    u32x4 b = {pack2(acc[8], acc[9]), pack2(acc[10], acc[11]), pack2(acc[12], acc[13]), pack2(acc[14], acc[15])};
    st16_wt(p.ws + WS_V, o, a);
    st16_wt(p.ws + WS_V, o + 512, b);
  }
}

constexpr u32 AT_RING = 0, AT_SLOT = 2304, AT_FLAG = AT_RING + 16 * AT_SLOT;
DEV void attn_wave(const Params& p, int head, int qb, int w, int lane) {
  const u32 ov = ((lane & 27) == 16) ? 0x3f803f80u : 0u;
  const char* kb = p.ws + WS_K + (size_t)(head * 64 + w * 8 + 4) * 1024;
  const char* vb = p.ws + WS_V + (size_t)(head * 64 + w * 8 + 4) * 1024;
  const size_t qa = (size_t)(p.ws + WS_Q + (size_t)(head * 64 + qb * 8) * 1024);
  const u32 ring = AT_RING + (w >> 2) * 8 * AT_SLOT;
  asm volatile(
  "v_mov_b32_e32 v80, %[ov]\n\t"
  "v_mov_b32_e32 v81, %[ov]\n\t"
  "v_mov_b64_e32 v[82:83], v[80:81]\n\t"
  "v_mov_b64_e32 v[84:85], v[80:81]\n\t"
  "v_mov_b64_e32 v[86:87], v[80:81]\n\t"
  "v_mov_b64_e32 v[88:89], v[80:81]\n\t"
  "v_mov_b64_e32 v[90:91], v[80:81]\n\t"
  "v_mov_b64_e32 v[92:93], v[80:81]\n\t"
  "v_mov_b64_e32 v[94:95], v[80:81]\n\t"
  "v_mov_b64_e32 v[96:97], v[80:81]\n\t"
  "v_mov_b64_e32 v[98:99], v[80:81]\n\t"
  "v_mov_b64_e32 v[100:101], v[80:81]\n\t"
  "v_mov_b64_e32 v[102:103], v[80:81]\n\t"
  "v_mov_b64_e32 v[104:105], v[80:81]\n\t"
  "v_mov_b64_e32 v[106:107], v[80:81]\n\t"
  "v_mov_b64_e32 v[108:109], v[80:81]\n\t"
  "v_mov_b64_e32 v[110:111], v[80:81]\n\t"
  "v_mov_b64_e32 v[112:113], v[80:81]\n\t"
  "v_mov_b64_e32 v[114:115], v[80:81]\n\t"
  "v_mov_b64_e32 v[116:117], v[80:81]\n\t"
  "v_mov_b64_e32 v[118:119], v[80:81]\n\t"
  "v_mov_b64_e32 v[120:121], v[80:81]\n\t"
  "v_mov_b64_e32 v[122:123], v[80:81]\n\t"
  "v_mov_b64_e32 v[124:125], v[80:81]\n\t"
  "v_mov_b64_e32 v[126:127], v[80:81]\n\t"
  "v_mov_b64_e32 v[128:129], v[80:81]\n\t"
  "v_mov_b64_e32 v[130:131], v[80:81]\n\t"
  "v_mov_b64_e32 v[132:133], v[80:81]\n\t"
  "v_mov_b64_e32 v[134:135], v[80:81]\n\t"
  "v_mov_b64_e32 v[136:137], v[80:81]\n\t"
  "v_mov_b64_e32 v[138:139], v[80:81]\n\t"
  "v_mov_b64_e32 v[140:141], v[80:81]\n\t"
  "v_mov_b64_e32 v[142:143], v[80:81]\n\t"
  "s_mov_b32 s70, %[p2]\n\t"
  "s_lshl_b32 s75, s70, 10\n\t"
  "s_add_u32 s72, %[qlo], s75\n\t"
  "s_addc_u32 s73, %[qhi], 0\n\t"
  "s_mov_b32 s76, 0x0000ffff\n\t"
  "s_mov_b32 s77, 0x0000ffff\n\t"
  "global_load_dwordx4 v[144:147], v157, s[72:73] sc1\n\t"
  "global_load_dwordx4 v[48:51], v157, %[kb] offset:-4096 sc1\n\t"
  "global_load_dwordx4 v[52:55], v157, %[kb] offset:-3072 sc1\n\t"
  "s_mov_b64 exec, s[76:77]\n\t"
  "global_load_dwordx4 v[80:83], v158, %[vb] offset:-4096 sc1\n\t"
  "global_load_dwordx4 v[84:87], v158, %[vb] offset:-3584 sc1\n\t"
  "s_mov_b64 exec, -1\n\t"
  "global_load_dwordx4 v[56:59], v157, %[kb] offset:-2048 sc1\n\t"
  "s_mov_b64 exec, s[76:77]\n\t"
  "global_load_dwordx4 v[88:91], v158, %[vb] offset:-3072 sc1\n\t"
  "global_load_dwordx4 v[92:95], v158, %[vb] offset:-2560 sc1\n\t"
  "s_mov_b64 exec, -1\n\t"
  "global_load_dwordx4 v[60:63], v157, %[kb] offset:-1024 sc1\n\t"
  "s_mov_b64 exec, s[76:77]\n\t"
  "global_load_dwordx4 v[96:99], v158, %[vb] offset:-2048 sc1\n\t"
  "global_load_dwordx4 v[100:103], v158, %[vb] offset:-1536 sc1\n\t"
  "s_mov_b64 exec, -1\n\t"
  "global_load_dwordx4 v[64:67], v157, %[kb] offset:0 sc1\n\t"
  "s_mov_b64 exec, s[76:77]\n\t"
  "global_load_dwordx4 v[104:107], v158, %[vb] offset:-1024 sc1\n\t"
  "global_load_dwordx4 v[108:111], v158, %[vb] offset:-512 sc1\n\t"
  "s_mov_b64 exec, -1\n\t"
  "global_load_dwordx4 v[68:71], v157, %[kb] offset:1024 sc1\n\t"
  "s_mov_b64 exec, s[76:77]\n\t"
  "global_load_dwordx4 v[112:115], v158, %[vb] offset:0 sc1\n\t"
  "global_load_dwordx4 v[116:119], v158, %[vb] offset:512 sc1\n\t"
  "s_mov_b64 exec, -1\n\t"
  "global_load_dwordx4 v[72:75], v157, %[kb] offset:2048 sc1\n\t"
  "s_mov_b64 exec, s[76:77]\n\t"
  "global_load_dwordx4 v[120:123], v158, %[vb] offset:1024 sc1\n\t"
  "global_load_dwordx4 v[124:127], v158, %[vb] offset:1536 sc1\n\t"
  "s_mov_b64 exec, -1\n\t"
  "global_load_dwordx4 v[76:79], v157, %[kb] offset:3072 sc1\n\t"
  "s_mov_b64 exec, s[76:77]\n\t"
  "global_load_dwordx4 v[128:131], v158, %[vb] offset:2048 sc1\n\t"
  "global_load_dwordx4 v[132:135], v158, %[vb] offset:2560 sc1\n\t"
  "s_mov_b64 exec, -1\n\t"
  "s_add_u32 s75, %[p2], 1\n\t"
  "s_lshl_b32 s75, s75, 10\n\t"
  "s_add_u32 s72, %[qlo], s75\n\t"
  "s_addc_u32 s73, %[qhi], 0\n\t"
  "global_load_dwordx4 v[148:151], v157, s[72:73] sc1\n\t"
  "s_mov_b64 exec, s[76:77]\n\t"
  "global_load_dwordx4 v[136:139], v158, %[vb] offset:3072 sc1\n\t"
  "global_load_dwordx4 v[140:143], v158, %[vb] offset:3584 sc1\n\t"
  "s_mov_b64 exec, -1\n\t"
  "v_mov_b64_e32 v[0:1], 0\n\t"
  "v_mov_b64_e32 v[2:3], 0\n\t"
  "v_mov_b64_e32 v[4:5], 0\n\t"
  "v_mov_b64_e32 v[6:7], 0\n\t"
  "v_mov_b64_e32 v[8:9], 0\n\t"
  "v_mov_b64_e32 v[10:11], 0\n\t"
  "v_mov_b64_e32 v[12:13], 0\n\t"
  "v_mov_b64_e32 v[14:15], 0\n\t"
  "s_mul_i32 s71, s70, 2304\n\t"
  "v_add_u32_e32 v160, s71, v153\n\t"
  "v_add_u32_e32 v161, s71, v154\n\t"
  "s_lshl_b32 s75, s70, 2\n\t"
  "v_add_u32_e32 v162, s75, v156\n\t"
  "v_mov_b32_e32 v159, 1\n\t"
  "s_mov_b32 s74, 0\n\t"
  "s_waitcnt vmcnt(24)\n\t"
  "v_mfma_f32_32x32x16_bf16 v[16:31], v[48:51], v[144:147], 0\n\t"
  "s_nop 11\n\t"
  "s_waitcnt vmcnt(22)\n\t"
  "v_exp_f32_e32 v16, v16\n\t"
  "v_exp_f32_e32 v17, v17\n\t"
  "v_exp_f32_e32 v18, v18\n\t"
  "v_exp_f32_e32 v19, v19\n\t"
  "v_exp_f32_e32 v20, v20\n\t"
  "v_mfma_f32_32x32x16_bf16 v[32:47], v[52:55], v[144:147], 0\n\t"
  "v_exp_f32_e32 v21, v21\n\t"
  "v_exp_f32_e32 v22, v22\n\t"
  "v_exp_f32_e32 v23, v23\n\t"
  "v_exp_f32_e32 v24, v24\n\t"
  "v_exp_f32_e32 v25, v25\n\t"
  "v_smfmac_f32_32x32x32_bf16 v[0:15], v[80:83], v[16:23], v152\n\t"
  "v_exp_f32_e32 v26, v26\n\t"
  "v_exp_f32_e32 v27, v27\n\t"
  "v_exp_f32_e32 v28, v28\n\t"
  "v_exp_f32_e32 v29, v29\n\t"
  "v_exp_f32_e32 v30, v30\n\t"
  "v_exp_f32_e32 v31, v31\n\t"
  "s_waitcnt vmcnt(19)\n\t"
  "v_exp_f32_e32 v32, v32\n\t"
  "v_exp_f32_e32 v33, v33\n\t"
  "v_smfmac_f32_32x32x32_bf16 v[0:15], v[84:87], v[24:31], v152\n\t"
  "v_exp_f32_e32 v34, v34\n\t"
  "v_exp_f32_e32 v35, v35\n\t"
  "v_exp_f32_e32 v36, v36\n\t"
  "v_mfma_f32_32x32x16_bf16 v[16:31], v[56:59], v[144:147], 0\n\t"
  "v_exp_f32_e32 v37, v37\n\t"
  "v_exp_f32_e32 v38, v38\n\t"
  "v_exp_f32_e32 v39, v39\n\t"
  "v_exp_f32_e32 v40, v40\n\t"
  "v_exp_f32_e32 v41, v41\n\t"
  "v_smfmac_f32_32x32x32_bf16 v[0:15], v[88:91], v[32:39], v152\n\t"
  "v_exp_f32_e32 v42, v42\n\t"
  "v_exp_f32_e32 v43, v43\n\t"
  "v_exp_f32_e32 v44, v44\n\t"
  "v_exp_f32_e32 v45, v45\n\t"
  "v_exp_f32_e32 v46, v46\n\t"
  "v_exp_f32_e32 v47, v47\n\t"
  "s_waitcnt vmcnt(16)\n\t"
  "v_exp_f32_e32 v16, v16\n\t"
  "v_exp_f32_e32 v17, v17\n\t"
  "v_smfmac_f32_32x32x32_bf16 v[0:15], v[92:95], v[40:47], v152\n\t"
  "v_exp_f32_e32 v18, v18\n\t"
  "v_exp_f32_e32 v19, v19\n\t"
  "v_exp_f32_e32 v20, v20\n\t"
  "v_mfma_f32_32x32x16_bf16 v[32:47], v[60:63], v[144:147], 0\n\t"
  "v_exp_f32_e32 v21, v21\n\t"
  "v_exp_f32_e32 v22, v22\n\t"
  "v_exp_f32_e32 v23, v23\n\t"
  "v_exp_f32_e32 v24, v24\n\t"
  "v_exp_f32_e32 v25, v25\n\t"
  "v_smfmac_f32_32x32x32_bf16 v[0:15], v[96:99], v[16:23], v152\n\t"
  "v_exp_f32_e32 v26, v26\n\t"
  "v_exp_f32_e32 v27, v27\n\t"
  "v_exp_f32_e32 v28, v28\n\t"
  "v_exp_f32_e32 v29, v29\n\t"
  "v_exp_f32_e32 v30, v30\n\t"
  "v_exp_f32_e32 v31, v31\n\t"
  "s_waitcnt vmcnt(13)\n\t"
  "v_exp_f32_e32 v32, v32\n\t"
  "v_exp_f32_e32 v33, v33\n\t"
  "v_smfmac_f32_32x32x32_bf16 v[0:15], v[100:103], v[24:31], v152\n\t"
  "v_exp_f32_e32 v34, v34\n\t"
  "v_exp_f32_e32 v35, v35\n\t"
  "v_exp_f32_e32 v36, v36\n\t"
  "v_mfma_f32_32x32x16_bf16 v[16:31], v[64:67], v[144:147], 0\n\t"
  "v_exp_f32_e32 v37, v37\n\t"
  "v_exp_f32_e32 v38, v38\n\t"
  "v_exp_f32_e32 v39, v39\n\t"
  "v_exp_f32_e32 v40, v40\n\t"
  "v_exp_f32_e32 v41, v41\n\t"
  "v_smfmac_f32_32x32x32_bf16 v[0:15], v[104:107], v[32:39], v152\n\t"
  "v_exp_f32_e32 v42, v42\n\t"
  "v_exp_f32_e32 v43, v43\n\t"
  "v_exp_f32_e32 v44, v44\n\t"
  "v_exp_f32_e32 v45, v45\n\t"
  "v_exp_f32_e32 v46, v46\n\t"
  "v_exp_f32_e32 v47, v47\n\t"
  "s_waitcnt vmcnt(10)\n\t"
  "v_exp_f32_e32 v16, v16\n\t"
  "v_exp_f32_e32 v17, v17\n\t"
  "v_smfmac_f32_32x32x32_bf16 v[0:15], v[108:111], v[40:47], v152\n\t"
  "v_exp_f32_e32 v18, v18\n\t"
  "v_exp_f32_e32 v19, v19\n\t"
  "v_exp_f32_e32 v20, v20\n\t"
  "v_mfma_f32_32x32x16_bf16 v[32:47], v[68:71], v[144:147], 0\n\t"
  "v_exp_f32_e32 v21, v21\n\t"
  "v_exp_f32_e32 v22, v22\n\t"
  "v_exp_f32_e32 v23, v23\n\t"
  "v_exp_f32_e32 v24, v24\n\t"
  "v_exp_f32_e32 v25, v25\n\t"
  "v_smfmac_f32_32x32x32_bf16 v[0:15], v[112:115], v[16:23], v152\n\t"
  "v_exp_f32_e32 v26, v26\n\t"
  "v_exp_f32_e32 v27, v27\n\t"
  "v_exp_f32_e32 v28, v28\n\t"
  "v_exp_f32_e32 v29, v29\n\t"
  "v_exp_f32_e32 v30, v30\n\t"
  "v_exp_f32_e32 v31, v31\n\t"
  "s_waitcnt vmcnt(7)\n\t"
  "v_exp_f32_e32 v32, v32\n\t"
  "v_exp_f32_e32 v33, v33\n\t"
  "v_smfmac_f32_32x32x32_bf16 v[0:15], v[116:119], v[24:31], v152\n\t"
  "v_exp_f32_e32 v34, v34\n\t"
  "v_exp_f32_e32 v35, v35\n\t"
  "v_exp_f32_e32 v36, v36\n\t"
  "v_mfma_f32_32x32x16_bf16 v[16:31], v[72:75], v[144:147], 0\n\t"
  "v_exp_f32_e32 v37, v37\n\t"
  "v_exp_f32_e32 v38, v38\n\t"
  "v_exp_f32_e32 v39, v39\n\t"
  "v_exp_f32_e32 v40, v40\n\t"
  "v_exp_f32_e32 v41, v41\n\t"
  "v_smfmac_f32_32x32x32_bf16 v[0:15], v[120:123], v[32:39], v152\n\t"
  "v_exp_f32_e32 v42, v42\n\t"
  "v_exp_f32_e32 v43, v43\n\t"
  "v_exp_f32_e32 v44, v44\n\t"
  "v_exp_f32_e32 v45, v45\n\t"
  "v_exp_f32_e32 v46, v46\n\t"
  "v_exp_f32_e32 v47, v47\n\t"
  "s_waitcnt vmcnt(4)\n\t"
  "v_exp_f32_e32 v16, v16\n\t"
  "v_exp_f32_e32 v17, v17\n\t"
  "v_smfmac_f32_32x32x32_bf16 v[0:15], v[124:127], v[40:47], v152\n\t"
  "v_exp_f32_e32 v18, v18\n\t"
  "v_exp_f32_e32 v19, v19\n\t"
  "v_exp_f32_e32 v20, v20\n\t"
  "v_mfma_f32_32x32x16_bf16 v[32:47], v[76:79], v[144:147], 0\n\t"
  "v_exp_f32_e32 v21, v21\n\t"
  "v_exp_f32_e32 v22, v22\n\t"
  "s_add_u32 s70, s70, 1\n\t"
  "s_and_b32 s70, s70, 7\n\t"
  "s_lshl_b32 s75, s70, 2\n\t"
  "v_add_u32_e32 v163, s75, v156\n\t"
  "v_exp_f32_e32 v23, v23\n\t"
  "v_exp_f32_e32 v24, v24\n\t"
  "v_exp_f32_e32 v25, v25\n\t"
  "v_smfmac_f32_32x32x32_bf16 v[0:15], v[128:131], v[16:23], v152\n\t"
  "v_exp_f32_e32 v26, v26\n\t"
  "v_exp_f32_e32 v27, v27\n\t"
  "v_exp_f32_e32 v28, v28\n\t"
  "v_exp_f32_e32 v29, v29\n\t"
  "v_exp_f32_e32 v30, v30\n\t"
  "v_exp_f32_e32 v31, v31\n\t"
  "s_waitcnt vmcnt(1)\n\t"
  "v_exp_f32_e32 v32, v32\n\t"
  "v_exp_f32_e32 v33, v33\n\t"
  "v_smfmac_f32_32x32x32_bf16 v[0:15], v[132:135], v[24:31], v152\n\t"
  "v_exp_f32_e32 v34, v34\n\t"
  "v_exp_f32_e32 v35, v35\n\t"
  "v_exp_f32_e32 v36, v36\n\t"
  "v_mfma_f32_32x32x16_bf16 v[16:31], v[48:51], v[148:151], 0\n\t"
  "v_exp_f32_e32 v37, v37\n\t"
  "v_exp_f32_e32 v38, v38\n\t"
  "ds_read_b32 v155, v163\n\t"
  "v_exp_f32_e32 v39, v39\n\t"
  "v_exp_f32_e32 v40, v40\n\t"
  "v_exp_f32_e32 v41, v41\n\t"
  "v_smfmac_f32_32x32x32_bf16 v[0:15], v[136:139], v[32:39], v152\n\t"
  "v_exp_f32_e32 v42, v42\n\t"
  "v_exp_f32_e32 v43, v43\n\t"
  "v_exp_f32_e32 v44, v44\n\t"
  "v_exp_f32_e32 v45, v45\n\t"
  "v_exp_f32_e32 v46, v46\n\t"
  "v_exp_f32_e32 v47, v47\n\t"
  "v_exp_f32_e32 v16, v16\n\t"
  "s_waitcnt vmcnt(0)\n\t"
  "v_exp_f32_e32 v17, v17\n\t"
  "v_smfmac_f32_32x32x32_bf16 v[0:15], v[140:143], v[40:47], v152\n\t"
  "v_exp_f32_e32 v18, v18\n\t"
  "s_add_u32 s70, s70, 1\n\t"
  "s_and_b32 s70, s70, 7\n\t"
  "s_lshl_b32 s75, s70, 10\n\t"
  "s_add_u32 s72, %[qlo], s75\n\t"
  "s_addc_u32 s73, %[qhi], 0\n\t"
  "v_exp_f32_e32 v19, v19\n\t"
  "global_load_dwordx4 v[144:147], v157, s[72:73] sc1\n\t"
  "v_exp_f32_e32 v20, v20\n\t"
  "v_mfma_f32_32x32x16_bf16 v[32:47], v[52:55], v[148:151], 0\n\t"
  "v_exp_f32_e32 v21, v21\n\t"
  "v_exp_f32_e32 v22, v22\n\t"
  "v_exp_f32_e32 v23, v23\n\t"
  "v_exp_f32_e32 v24, v24\n\t"
  "v_exp_f32_e32 v25, v25\n\t"
  "v_exp_f32_e32 v26, v26\n\t"
  "v_exp_f32_e32 v27, v27\n\t"
  "v_exp_f32_e32 v28, v28\n\t"
  "ds_write_b128 v160, v[0:3]\n\t"
  "ds_write_b128 v160, v[4:7] offset:1024\n\t"
  "ds_write_b32 v161, v8\n\t"
  "s_mov_b64 exec, 1\n\t"
  "ds_write_b32 v162, v159\n\t"
  "s_mov_b64 exec, -1\n\t"
  "s_sub_u32 s75, s70, 1\n\t"
  "s_and_b32 s75, s75, 7\n\t"
  "s_mul_i32 s71, s75, 2304\n\t"
  "v_add_u32_e32 v160, s71, v153\n\t"
  "v_add_u32_e32 v161, s71, v154\n\t"
  "s_nop 1\n\t"
  "v_mov_b64_e32 v[0:1], 0\n\t"
  "v_mov_b64_e32 v[2:3], 0\n\t"
  "v_mov_b64_e32 v[4:5], 0\n\t"
  "v_mov_b64_e32 v[6:7], 0\n\t"
  "v_mov_b32_e32 v8, 0\n\t"
  "v_exp_f32_e32 v29, v29\n\t"
  "v_exp_f32_e32 v30, v30\n\t"
  "v_exp_f32_e32 v31, v31\n\t"
  "v_exp_f32_e32 v32, v32\n\t"
  "v_smfmac_f32_32x32x32_bf16 v[0:15], v[80:83], v[16:23], v152\n\t"
  "v_exp_f32_e32 v33, v33\n\t"
  "v_exp_f32_e32 v34, v34\n\t"
  "v_smfmac_f32_32x32x32_bf16 v[0:15], v[84:87], v[24:31], v152\n\t"
  "v_exp_f32_e32 v35, v35\n\t"
  "v_exp_f32_e32 v36, v36\n\t"
  "v_mfma_f32_32x32x16_bf16 v[16:31], v[56:59], v[148:151], 0\n\t"
  "v_exp_f32_e32 v37, v37\n\t"
  "v_exp_f32_e32 v38, v38\n\t"
  "v_exp_f32_e32 v39, v39\n\t"
  "v_exp_f32_e32 v40, v40\n\t"
  "v_exp_f32_e32 v41, v41\n\t"
  "v_smfmac_f32_32x32x32_bf16 v[0:15], v[88:91], v[32:39], v152\n\t"
  "v_exp_f32_e32 v42, v42\n\t"
  "v_exp_f32_e32 v43, v43\n\t"
  "v_exp_f32_e32 v44, v44\n\t"
  "v_exp_f32_e32 v45, v45\n\t"
  "v_exp_f32_e32 v46, v46\n\t"
  "v_exp_f32_e32 v47, v47\n\t"
  "v_exp_f32_e32 v16, v16\n\t"
  "v_exp_f32_e32 v17, v17\n\t"
  "v_smfmac_f32_32x32x32_bf16 v[0:15], v[92:95], v[40:47], v152\n\t"
  "v_exp_f32_e32 v18, v18\n\t"
  "v_exp_f32_e32 v19, v19\n\t"
  "v_exp_f32_e32 v20, v20\n\t"
  "v_mfma_f32_32x32x16_bf16 v[32:47], v[60:63], v[148:151], 0\n\t"
  "v_exp_f32_e32 v21, v21\n\t"
  "v_exp_f32_e32 v22, v22\n\t"
  "v_exp_f32_e32 v23, v23\n\t"
  "v_exp_f32_e32 v24, v24\n\t"
  "v_exp_f32_e32 v25, v25\n\t"
  "v_smfmac_f32_32x32x32_bf16 v[0:15], v[96:99], v[16:23], v152\n\t"
  "v_exp_f32_e32 v26, v26\n\t"
  "v_exp_f32_e32 v27, v27\n\t"
  "v_exp_f32_e32 v28, v28\n\t"
  "v_exp_f32_e32 v29, v29\n\t"
  "v_exp_f32_e32 v30, v30\n\t"
  "v_exp_f32_e32 v31, v31\n\t"
  "v_exp_f32_e32 v32, v32\n\t"
  "v_exp_f32_e32 v33, v33\n\t"
  "v_smfmac_f32_32x32x32_bf16 v[0:15], v[100:103], v[24:31], v152\n\t"
  "v_exp_f32_e32 v34, v34\n\t"
  "v_exp_f32_e32 v35, v35\n\t"
  "v_exp_f32_e32 v36, v36\n\t"
  "v_mfma_f32_32x32x16_bf16 v[16:31], v[64:67], v[148:151], 0\n\t"
  "v_exp_f32_e32 v37, v37\n\t"
  "v_exp_f32_e32 v38, v38\n\t"
  "v_exp_f32_e32 v39, v39\n\t"
  "v_exp_f32_e32 v40, v40\n\t"
  "v_exp_f32_e32 v41, v41\n\t"
  "v_smfmac_f32_32x32x32_bf16 v[0:15], v[104:107], v[32:39], v152\n\t"
  "v_exp_f32_e32 v42, v42\n\t"
  "v_exp_f32_e32 v43, v43\n\t"
  "v_exp_f32_e32 v44, v44\n\t"
  "v_exp_f32_e32 v45, v45\n\t"
  "v_exp_f32_e32 v46, v46\n\t"
  "v_exp_f32_e32 v47, v47\n\t"
  "v_exp_f32_e32 v16, v16\n\t"
  "v_exp_f32_e32 v17, v17\n\t"
  "v_smfmac_f32_32x32x32_bf16 v[0:15], v[108:111], v[40:47], v152\n\t"
  "v_exp_f32_e32 v18, v18\n\t"
  "v_exp_f32_e32 v19, v19\n\t"
  "v_exp_f32_e32 v20, v20\n\t"
  "v_mfma_f32_32x32x16_bf16 v[32:47], v[68:71], v[148:151], 0\n\t"
  "v_exp_f32_e32 v21, v21\n\t"
  "v_exp_f32_e32 v22, v22\n\t"
  "v_exp_f32_e32 v23, v23\n\t"
  "v_exp_f32_e32 v24, v24\n\t"
  "v_exp_f32_e32 v25, v25\n\t"
  "v_smfmac_f32_32x32x32_bf16 v[0:15], v[112:115], v[16:23], v152\n\t"
  "v_exp_f32_e32 v26, v26\n\t"
  "v_exp_f32_e32 v27, v27\n\t"
  "v_exp_f32_e32 v28, v28\n\t"
  "v_exp_f32_e32 v29, v29\n\t"
  "v_exp_f32_e32 v30, v30\n\t"
  "v_exp_f32_e32 v31, v31\n\t"
  "v_exp_f32_e32 v32, v32\n\t"
  "v_exp_f32_e32 v33, v33\n\t"
  "v_smfmac_f32_32x32x32_bf16 v[0:15], v[116:119], v[24:31], v152\n\t"
  "v_exp_f32_e32 v34, v34\n\t"
  "v_exp_f32_e32 v35, v35\n\t"
  "v_exp_f32_e32 v36, v36\n\t"
  "v_mfma_f32_32x32x16_bf16 v[16:31], v[72:75], v[148:151], 0\n\t"
  "v_exp_f32_e32 v37, v37\n\t"
  "v_exp_f32_e32 v38, v38\n\t"
  "v_exp_f32_e32 v39, v39\n\t"
  "v_exp_f32_e32 v40, v40\n\t"
  "v_exp_f32_e32 v41, v41\n\t"
  "v_smfmac_f32_32x32x32_bf16 v[0:15], v[120:123], v[32:39], v152\n\t"
  "v_exp_f32_e32 v42, v42\n\t"
  "v_exp_f32_e32 v43, v43\n\t"
  "v_exp_f32_e32 v44, v44\n\t"
  "v_exp_f32_e32 v45, v45\n\t"
  "v_exp_f32_e32 v46, v46\n\t"
  "v_exp_f32_e32 v47, v47\n\t"
  "v_exp_f32_e32 v16, v16\n\t"
  "v_exp_f32_e32 v17, v17\n\t"
  "v_smfmac_f32_32x32x32_bf16 v[0:15], v[124:127], v[40:47], v152\n\t"
  "v_exp_f32_e32 v18, v18\n\t"
  "v_exp_f32_e32 v19, v19\n\t"
  "v_exp_f32_e32 v20, v20\n\t"
  "v_mfma_f32_32x32x16_bf16 v[32:47], v[76:79], v[148:151], 0\n\t"
  "v_exp_f32_e32 v21, v21\n\t"
  "v_exp_f32_e32 v22, v22\n\t"
  "s_lshl_b32 s75, s70, 2\n\t"
  "v_add_u32_e32 v162, s75, v156\n\t"
  "v_exp_f32_e32 v23, v23\n\t"
  "v_exp_f32_e32 v24, v24\n\t"
  "v_exp_f32_e32 v25, v25\n\t"
  "v_smfmac_f32_32x32x32_bf16 v[0:15], v[128:131], v[16:23], v152\n\t"
  "v_exp_f32_e32 v26, v26\n\t"
  "v_exp_f32_e32 v27, v27\n\t"
  "v_exp_f32_e32 v28, v28\n\t"
  "v_exp_f32_e32 v29, v29\n\t"
  "v_exp_f32_e32 v30, v30\n\t"
  "v_exp_f32_e32 v31, v31\n\t"
  "v_exp_f32_e32 v32, v32\n\t"
  "v_exp_f32_e32 v33, v33\n\t"
  "v_smfmac_f32_32x32x32_bf16 v[0:15], v[132:135], v[24:31], v152\n\t"
  "v_exp_f32_e32 v34, v34\n\t"
  "v_exp_f32_e32 v35, v35\n\t"
  "v_exp_f32_e32 v36, v36\n\t"
  "s_waitcnt vmcnt(0)\n\t"
  "v_mfma_f32_32x32x16_bf16 v[16:31], v[48:51], v[144:147], 0\n\t"
  "v_exp_f32_e32 v37, v37\n\t"
  "v_exp_f32_e32 v38, v38\n\t"
  "ds_read_b32 v155, v162\n\t"
  "v_exp_f32_e32 v39, v39\n\t"
  "v_exp_f32_e32 v40, v40\n\t"
  "v_exp_f32_e32 v41, v41\n\t"
  "v_smfmac_f32_32x32x32_bf16 v[0:15], v[136:139], v[32:39], v152\n\t"
  "v_exp_f32_e32 v42, v42\n\t"
  "v_exp_f32_e32 v43, v43\n\t"
  "v_exp_f32_e32 v44, v44\n\t"
  "v_exp_f32_e32 v45, v45\n\t"
  "v_exp_f32_e32 v46, v46\n\t"
  "v_exp_f32_e32 v47, v47\n\t"
  "s_mov_b32 s74, 1\n\t"
  "Lloop_%=:\n\t"
  "v_exp_f32_e32 v16, v16\n\t"
  "v_exp_f32_e32 v17, v17\n\t"
  "v_smfmac_f32_32x32x32_bf16 v[0:15], v[140:143], v[40:47], v152\n\t"
  "v_exp_f32_e32 v18, v18\n\t"
  "s_add_u32 s70, s70, 1\n\t"
  "s_and_b32 s70, s70, 7\n\t"
  "s_lshl_b32 s75, s70, 10\n\t"
  "s_add_u32 s72, %[qlo], s75\n\t"
  "s_addc_u32 s73, %[qhi], 0\n\t"
  "v_exp_f32_e32 v19, v19\n\t"
  "global_load_dwordx4 v[148:151], v157, s[72:73] sc1\n\t"
  "v_exp_f32_e32 v20, v20\n\t"
  "v_mfma_f32_32x32x16_bf16 v[32:47], v[52:55], v[144:147], 0\n\t"
  "v_exp_f32_e32 v21, v21\n\t"
  "v_exp_f32_e32 v22, v22\n\t"
  "v_exp_f32_e32 v23, v23\n\t"
  "v_exp_f32_e32 v24, v24\n\t"
  "v_exp_f32_e32 v25, v25\n\t"
  "v_exp_f32_e32 v26, v26\n\t"
  "v_exp_f32_e32 v27, v27\n\t"
  "v_exp_f32_e32 v28, v28\n\t"
  "ds_write_b128 v160, v[0:3]\n\t"
  "ds_write_b128 v160, v[4:7] offset:1024\n\t"
  "ds_write_b32 v161, v8\n\t"
  "s_mov_b64 exec, 1\n\t"
  "ds_write_b32 v163, v159\n\t"
  "s_mov_b64 exec, -1\n\t"
  "s_sub_u32 s75, s70, 1\n\t"
  "s_and_b32 s75, s75, 7\n\t"
  "s_mul_i32 s71, s75, 2304\n\t"
  "v_add_u32_e32 v160, s71, v153\n\t"
  "v_add_u32_e32 v161, s71, v154\n\t"
  "s_waitcnt lgkmcnt(4)\n\t"
  "v_readfirstlane_b32 s75, v155\n\t"
  "s_cmp_ge_u32 s75, s74\n\t"
  "s_cbranch_scc1 Lrdy1_%=\n\t"
  "Lspin1_%=:\n\t"
  "s_sleep 2\n\t"
  "ds_read_b32 v155, v162\n\t"
  "s_waitcnt lgkmcnt(0)\n\t"
  "v_readfirstlane_b32 s75, v155\n\t"
  "s_cmp_ge_u32 s75, s74\n\t"
  "s_cbranch_scc0 Lspin1_%=\n\t"
  "Lrdy1_%=:\n\t"
  "ds_read_b128 v[0:3], v160\n\t"
  "ds_read_b128 v[4:7], v160 offset:1024\n\t"
  "ds_read_b32 v8, v161\n\t"
  "s_add_u32 s75, s74, 1\n\t"
  "v_mov_b32_e32 v159, s75\n\t"
  "v_exp_f32_e32 v29, v29\n\t"
  "v_exp_f32_e32 v30, v30\n\t"
  "v_exp_f32_e32 v31, v31\n\t"
  "v_exp_f32_e32 v32, v32\n\t"
  "s_waitcnt lgkmcnt(0)\n\t"
  "v_smfmac_f32_32x32x32_bf16 v[0:15], v[80:83], v[16:23], v152\n\t"
  "v_exp_f32_e32 v33, v33\n\t"
  "v_exp_f32_e32 v34, v34\n\t"
  "v_smfmac_f32_32x32x32_bf16 v[0:15], v[84:87], v[24:31], v152\n\t"
  "v_exp_f32_e32 v35, v35\n\t"
  "v_exp_f32_e32 v36, v36\n\t"
  "v_mfma_f32_32x32x16_bf16 v[16:31], v[56:59], v[144:147], 0\n\t"
  "v_exp_f32_e32 v37, v37\n\t"
  "v_exp_f32_e32 v38, v38\n\t"
  "v_exp_f32_e32 v39, v39\n\t"
  "v_exp_f32_e32 v40, v40\n\t"
  "v_exp_f32_e32 v41, v41\n\t"
  "v_smfmac_f32_32x32x32_bf16 v[0:15], v[88:91], v[32:39], v152\n\t"
  "v_exp_f32_e32 v42, v42\n\t"
  "v_exp_f32_e32 v43, v43\n\t"
  "v_exp_f32_e32 v44, v44\n\t"
  "v_exp_f32_e32 v45, v45\n\t"
  "v_exp_f32_e32 v46, v46\n\t"
  "v_exp_f32_e32 v47, v47\n\t"
  "v_exp_f32_e32 v16, v16\n\t"
  "v_exp_f32_e32 v17, v17\n\t"
  "v_smfmac_f32_32x32x32_bf16 v[0:15], v[92:95], v[40:47], v152\n\t"
  "v_exp_f32_e32 v18, v18\n\t"
  "v_exp_f32_e32 v19, v19\n\t"
  "v_exp_f32_e32 v20, v20\n\t"
  "v_mfma_f32_32x32x16_bf16 v[32:47], v[60:63], v[144:147], 0\n\t"
  "v_exp_f32_e32 v21, v21\n\t"
  "v_exp_f32_e32 v22, v22\n\t"
  "v_exp_f32_e32 v23, v23\n\t"
  "v_exp_f32_e32 v24, v24\n\t"
  "v_exp_f32_e32 v25, v25\n\t"
  "v_smfmac_f32_32x32x32_bf16 v[0:15], v[96:99], v[16:23], v152\n\t"
  "v_exp_f32_e32 v26, v26\n\t"
  "v_exp_f32_e32 v27, v27\n\t"
  "v_exp_f32_e32 v28, v28\n\t"
  "v_exp_f32_e32 v29, v29\n\t"
  "v_exp_f32_e32 v30, v30\n\t"
  "v_exp_f32_e32 v31, v31\n\t"
  "v_exp_f32_e32 v32, v32\n\t"
  "v_exp_f32_e32 v33, v33\n\t"
  "v_smfmac_f32_32x32x32_bf16 v[0:15], v[100:103], v[24:31], v152\n\t"
  "v_exp_f32_e32 v34, v34\n\t"
  "v_exp_f32_e32 v35, v35\n\t"
  "v_exp_f32_e32 v36, v36\n\t"
  "v_mfma_f32_32x32x16_bf16 v[16:31], v[64:67], v[144:147], 0\n\t"
  "v_exp_f32_e32 v37, v37\n\t"
  "v_exp_f32_e32 v38, v38\n\t"
  "v_exp_f32_e32 v39, v39\n\t"
  "v_exp_f32_e32 v40, v40\n\t"
  "v_exp_f32_e32 v41, v41\n\t"
  "v_smfmac_f32_32x32x32_bf16 v[0:15], v[104:107], v[32:39], v152\n\t"
  "v_exp_f32_e32 v42, v42\n\t"
  "v_exp_f32_e32 v43, v43\n\t"
  "v_exp_f32_e32 v44, v44\n\t"
  "v_exp_f32_e32 v45, v45\n\t"
  "v_exp_f32_e32 v46, v46\n\t"
  "v_exp_f32_e32 v47, v47\n\t"
  "v_exp_f32_e32 v16, v16\n\t"
  "v_exp_f32_e32 v17, v17\n\t"
  "v_smfmac_f32_32x32x32_bf16 v[0:15], v[108:111], v[40:47], v152\n\t"
  "v_exp_f32_e32 v18, v18\n\t"
  "v_exp_f32_e32 v19, v19\n\t"
  "v_exp_f32_e32 v20, v20\n\t"
  "v_mfma_f32_32x32x16_bf16 v[32:47], v[68:71], v[144:147], 0\n\t"
  "v_exp_f32_e32 v21, v21\n\t"
  "v_exp_f32_e32 v22, v22\n\t"
  "v_exp_f32_e32 v23, v23\n\t"
  "v_exp_f32_e32 v24, v24\n\t"
  "v_exp_f32_e32 v25, v25\n\t"
  "v_smfmac_f32_32x32x32_bf16 v[0:15], v[112:115], v[16:23], v152\n\t"
  "v_exp_f32_e32 v26, v26\n\t"
  "v_exp_f32_e32 v27, v27\n\t"
  "v_exp_f32_e32 v28, v28\n\t"
  "v_exp_f32_e32 v29, v29\n\t"
  "v_exp_f32_e32 v30, v30\n\t"
  "v_exp_f32_e32 v31, v31\n\t"
  "v_exp_f32_e32 v32, v32\n\t"
  "v_exp_f32_e32 v33, v33\n\t"
  "v_smfmac_f32_32x32x32_bf16 v[0:15], v[116:119], v[24:31], v152\n\t"
  "v_exp_f32_e32 v34, v34\n\t"
  "v_exp_f32_e32 v35, v35\n\t"
  "v_exp_f32_e32 v36, v36\n\t"
  "v_mfma_f32_32x32x16_bf16 v[16:31], v[72:75], v[144:147], 0\n\t"
  "v_exp_f32_e32 v37, v37\n\t"
  "v_exp_f32_e32 v38, v38\n\t"
  "v_exp_f32_e32 v39, v39\n\t"
  "v_exp_f32_e32 v40, v40\n\t"
  "v_exp_f32_e32 v41, v41\n\t"
  "v_smfmac_f32_32x32x32_bf16 v[0:15], v[120:123], v[32:39], v152\n\t"
  "v_exp_f32_e32 v42, v42\n\t"
  "v_exp_f32_e32 v43, v43\n\t"
  "v_exp_f32_e32 v44, v44\n\t"
  "v_exp_f32_e32 v45, v45\n\t"
  "v_exp_f32_e32 v46, v46\n\t"
  "v_exp_f32_e32 v47, v47\n\t"
  "v_exp_f32_e32 v16, v16\n\t"
  "v_exp_f32_e32 v17, v17\n\t"
  "v_smfmac_f32_32x32x32_bf16 v[0:15], v[124:127], v[40:47], v152\n\t"
  "v_exp_f32_e32 v18, v18\n\t"
  "v_exp_f32_e32 v19, v19\n\t"
  "v_exp_f32_e32 v20, v20\n\t"
  "v_mfma_f32_32x32x16_bf16 v[32:47], v[76:79], v[144:147], 0\n\t"
  "v_exp_f32_e32 v21, v21\n\t"
  "v_exp_f32_e32 v22, v22\n\t"
  "s_lshl_b32 s75, s70, 2\n\t"
  "v_add_u32_e32 v163, s75, v156\n\t"
  "v_exp_f32_e32 v23, v23\n\t"
  "v_exp_f32_e32 v24, v24\n\t"
  "v_exp_f32_e32 v25, v25\n\t"
  "v_smfmac_f32_32x32x32_bf16 v[0:15], v[128:131], v[16:23], v152\n\t"
  "v_exp_f32_e32 v26, v26\n\t"
  "v_exp_f32_e32 v27, v27\n\t"
  "v_exp_f32_e32 v28, v28\n\t"
  "v_exp_f32_e32 v29, v29\n\t"
  "v_exp_f32_e32 v30, v30\n\t"
  "v_exp_f32_e32 v31, v31\n\t"
  "v_exp_f32_e32 v32, v32\n\t"
  "v_exp_f32_e32 v33, v33\n\t"
  "v_smfmac_f32_32x32x32_bf16 v[0:15], v[132:135], v[24:31], v152\n\t"
  "v_exp_f32_e32 v34, v34\n\t"
  "v_exp_f32_e32 v35, v35\n\t"
  "v_exp_f32_e32 v36, v36\n\t"
  "s_waitcnt vmcnt(0)\n\t"
  "v_mfma_f32_32x32x16_bf16 v[16:31], v[48:51], v[148:151], 0\n\t"
  "v_exp_f32_e32 v37, v37\n\t"
  "v_exp_f32_e32 v38, v38\n\t"
  "ds_read_b32 v155, v163\n\t"
  "v_exp_f32_e32 v39, v39\n\t"
  "v_exp_f32_e32 v40, v40\n\t"
  "v_exp_f32_e32 v41, v41\n\t"
  "v_smfmac_f32_32x32x32_bf16 v[0:15], v[136:139], v[32:39], v152\n\t"
  "v_exp_f32_e32 v42, v42\n\t"
  "v_exp_f32_e32 v43, v43\n\t"
  "v_exp_f32_e32 v44, v44\n\t"
  "v_exp_f32_e32 v45, v45\n\t"
  "v_exp_f32_e32 v46, v46\n\t"
  "v_exp_f32_e32 v47, v47\n\t"
  "s_cmp_eq_u32 s74, 3\n\t"
  "s_cbranch_scc1 Llast_%=\n\t"
  "v_exp_f32_e32 v16, v16\n\t"
  "v_exp_f32_e32 v17, v17\n\t"
  "v_smfmac_f32_32x32x32_bf16 v[0:15], v[140:143], v[40:47], v152\n\t"
  "v_exp_f32_e32 v18, v18\n\t"
  "s_add_u32 s70, s70, 1\n\t"
  "s_and_b32 s70, s70, 7\n\t"
  "s_lshl_b32 s75, s70, 10\n\t"
  "s_add_u32 s72, %[qlo], s75\n\t"
  "s_addc_u32 s73, %[qhi], 0\n\t"
  "v_exp_f32_e32 v19, v19\n\t"
  "global_load_dwordx4 v[144:147], v157, s[72:73] sc1\n\t"
  "v_exp_f32_e32 v20, v20\n\t"
  "v_mfma_f32_32x32x16_bf16 v[32:47], v[52:55], v[148:151], 0\n\t"
  "v_exp_f32_e32 v21, v21\n\t"
  "v_exp_f32_e32 v22, v22\n\t"
  "v_exp_f32_e32 v23, v23\n\t"
  "v_exp_f32_e32 v24, v24\n\t"
  "v_exp_f32_e32 v25, v25\n\t"
  "v_exp_f32_e32 v26, v26\n\t"
  "v_exp_f32_e32 v27, v27\n\t"
  "v_exp_f32_e32 v28, v28\n\t"
  "ds_write_b128 v160, v[0:3]\n\t"
  "ds_write_b128 v160, v[4:7] offset:1024\n\t"
  "ds_write_b32 v161, v8\n\t"
  "s_mov_b64 exec, 1\n\t"
  "ds_write_b32 v162, v159\n\t"
  "s_mov_b64 exec, -1\n\t"
  "s_sub_u32 s75, s70, 1\n\t"
  "s_and_b32 s75, s75, 7\n\t"
  "s_mul_i32 s71, s75, 2304\n\t"
  "v_add_u32_e32 v160, s71, v153\n\t"
  "v_add_u32_e32 v161, s71, v154\n\t"
  "s_waitcnt lgkmcnt(4)\n\t"
  "v_readfirstlane_b32 s75, v155\n\t"
  "s_cmp_ge_u32 s75, s74\n\t"
  "s_cbranch_scc1 Lrdy2_%=\n\t"
  "Lspin2_%=:\n\t"
  "s_sleep 2\n\t"
  "ds_read_b32 v155, v163\n\t"
  "s_waitcnt lgkmcnt(0)\n\t"
  "v_readfirstlane_b32 s75, v155\n\t"
  "s_cmp_ge_u32 s75, s74\n\t"
  "s_cbranch_scc0 Lspin2_%=\n\t"
  "Lrdy2_%=:\n\t"
  "ds_read_b128 v[0:3], v160\n\t"
  "ds_read_b128 v[4:7], v160 offset:1024\n\t"
  "ds_read_b32 v8, v161\n\t"
  "v_exp_f32_e32 v29, v29\n\t"
  "v_exp_f32_e32 v30, v30\n\t"
  "v_exp_f32_e32 v31, v31\n\t"
  "v_exp_f32_e32 v32, v32\n\t"
  "s_waitcnt lgkmcnt(0)\n\t"
  "v_smfmac_f32_32x32x32_bf16 v[0:15], v[80:83], v[16:23], v152\n\t"
  "v_exp_f32_e32 v33, v33\n\t"
  "v_exp_f32_e32 v34, v34\n\t"
  "v_smfmac_f32_32x32x32_bf16 v[0:15], v[84:87], v[24:31], v152\n\t"
  "v_exp_f32_e32 v35, v35\n\t"
  "v_exp_f32_e32 v36, v36\n\t"
  "v_mfma_f32_32x32x16_bf16 v[16:31], v[56:59], v[148:151], 0\n\t"
  "v_exp_f32_e32 v37, v37\n\t"
  "v_exp_f32_e32 v38, v38\n\t"
  "v_exp_f32_e32 v39, v39\n\t"
  "v_exp_f32_e32 v40, v40\n\t"
  "v_exp_f32_e32 v41, v41\n\t"
  "v_smfmac_f32_32x32x32_bf16 v[0:15], v[88:91], v[32:39], v152\n\t"
  "v_exp_f32_e32 v42, v42\n\t"
  "v_exp_f32_e32 v43, v43\n\t"
  "v_exp_f32_e32 v44, v44\n\t"
  "v_exp_f32_e32 v45, v45\n\t"
  "v_exp_f32_e32 v46, v46\n\t"
  "v_exp_f32_e32 v47, v47\n\t"
  "v_exp_f32_e32 v16, v16\n\t"
  "v_exp_f32_e32 v17, v17\n\t"
  "v_smfmac_f32_32x32x32_bf16 v[0:15], v[92:95], v[40:47], v152\n\t"
  "v_exp_f32_e32 v18, v18\n\t"
  "v_exp_f32_e32 v19, v19\n\t"
  "v_exp_f32_e32 v20, v20\n\t"
  "v_mfma_f32_32x32x16_bf16 v[32:47], v[60:63], v[148:151], 0\n\t"
  "v_exp_f32_e32 v21, v21\n\t"
  "v_exp_f32_e32 v22, v22\n\t"
  "v_exp_f32_e32 v23, v23\n\t"
  "v_exp_f32_e32 v24, v24\n\t"
  "v_exp_f32_e32 v25, v25\n\t"
  "v_smfmac_f32_32x32x32_bf16 v[0:15], v[96:99], v[16:23], v152\n\t"
  "v_exp_f32_e32 v26, v26\n\t"
  "v_exp_f32_e32 v27, v27\n\t"
  "v_exp_f32_e32 v28, v28\n\t"
  "v_exp_f32_e32 v29, v29\n\t"
  "v_exp_f32_e32 v30, v30\n\t"
  "v_exp_f32_e32 v31, v31\n\t"
  "v_exp_f32_e32 v32, v32\n\t"
  "v_exp_f32_e32 v33, v33\n\t"
  "v_smfmac_f32_32x32x32_bf16 v[0:15], v[100:103], v[24:31], v152\n\t"
  "v_exp_f32_e32 v34, v34\n\t"
  "v_exp_f32_e32 v35, v35\n\t"
  "v_exp_f32_e32 v36, v36\n\t"
  "v_mfma_f32_32x32x16_bf16 v[16:31], v[64:67], v[148:151], 0\n\t"
  "v_exp_f32_e32 v37, v37\n\t"
  "v_exp_f32_e32 v38, v38\n\t"
  "v_exp_f32_e32 v39, v39\n\t"
  "v_exp_f32_e32 v40, v40\n\t"
  "v_exp_f32_e32 v41, v41\n\t"
  "v_smfmac_f32_32x32x32_bf16 v[0:15], v[104:107], v[32:39], v152\n\t"
  "v_exp_f32_e32 v42, v42\n\t"
  "v_exp_f32_e32 v43, v43\n\t"
  "v_exp_f32_e32 v44, v44\n\t"
  "v_exp_f32_e32 v45, v45\n\t"
  "v_exp_f32_e32 v46, v46\n\t"
  "v_exp_f32_e32 v47, v47\n\t"
  "v_exp_f32_e32 v16, v16\n\t"
  "v_exp_f32_e32 v17, v17\n\t"
  "v_smfmac_f32_32x32x32_bf16 v[0:15], v[108:111], v[40:47], v152\n\t"
  "v_exp_f32_e32 v18, v18\n\t"
  "v_exp_f32_e32 v19, v19\n\t"
  "v_exp_f32_e32 v20, v20\n\t"
  "v_mfma_f32_32x32x16_bf16 v[32:47], v[68:71], v[148:151], 0\n\t"
  "v_exp_f32_e32 v21, v21\n\t"
  "v_exp_f32_e32 v22, v22\n\t"
  "v_exp_f32_e32 v23, v23\n\t"
  "v_exp_f32_e32 v24, v24\n\t"
  "v_exp_f32_e32 v25, v25\n\t"
  "v_smfmac_f32_32x32x32_bf16 v[0:15], v[112:115], v[16:23], v152\n\t"
  "v_exp_f32_e32 v26, v26\n\t"
  "v_exp_f32_e32 v27, v27\n\t"
  "v_exp_f32_e32 v28, v28\n\t"
  "v_exp_f32_e32 v29, v29\n\t"
  "v_exp_f32_e32 v30, v30\n\t"
  "v_exp_f32_e32 v31, v31\n\t"
  "v_exp_f32_e32 v32, v32\n\t"
  "v_exp_f32_e32 v33, v33\n\t"
  "v_smfmac_f32_32x32x32_bf16 v[0:15], v[116:119], v[24:31], v152\n\t"
  "v_exp_f32_e32 v34, v34\n\t"
  "v_exp_f32_e32 v35, v35\n\t"
  "v_exp_f32_e32 v36, v36\n\t"
  "v_mfma_f32_32x32x16_bf16 v[16:31], v[72:75], v[148:151], 0\n\t"
  "v_exp_f32_e32 v37, v37\n\t"
  "v_exp_f32_e32 v38, v38\n\t"
  "v_exp_f32_e32 v39, v39\n\t"
  "v_exp_f32_e32 v40, v40\n\t"
  "v_exp_f32_e32 v41, v41\n\t"
  "v_smfmac_f32_32x32x32_bf16 v[0:15], v[120:123], v[32:39], v152\n\t"
  "v_exp_f32_e32 v42, v42\n\t"
  "v_exp_f32_e32 v43, v43\n\t"
  "v_exp_f32_e32 v44, v44\n\t"
  "v_exp_f32_e32 v45, v45\n\t"
  "v_exp_f32_e32 v46, v46\n\t"
  "v_exp_f32_e32 v47, v47\n\t"
  "v_exp_f32_e32 v16, v16\n\t"
  "v_exp_f32_e32 v17, v17\n\t"
  "v_smfmac_f32_32x32x32_bf16 v[0:15], v[124:127], v[40:47], v152\n\t"
  "v_exp_f32_e32 v18, v18\n\t"
  "v_exp_f32_e32 v19, v19\n\t"
  "v_exp_f32_e32 v20, v20\n\t"
  "v_mfma_f32_32x32x16_bf16 v[32:47], v[76:79], v[148:151], 0\n\t"
  "v_exp_f32_e32 v21, v21\n\t"
  "v_exp_f32_e32 v22, v22\n\t"
  "s_lshl_b32 s75, s70, 2\n\t"
  "v_add_u32_e32 v162, s75, v156\n\t"
  "v_exp_f32_e32 v23, v23\n\t"
  "v_exp_f32_e32 v24, v24\n\t"
  "v_exp_f32_e32 v25, v25\n\t"
  "v_smfmac_f32_32x32x32_bf16 v[0:15], v[128:131], v[16:23], v152\n\t"
  "v_exp_f32_e32 v26, v26\n\t"
  "v_exp_f32_e32 v27, v27\n\t"
  "v_exp_f32_e32 v28, v28\n\t"
  "v_exp_f32_e32 v29, v29\n\t"
  "v_exp_f32_e32 v30, v30\n\t"
  "v_exp_f32_e32 v31, v31\n\t"
  "v_exp_f32_e32 v32, v32\n\t"
  "v_exp_f32_e32 v33, v33\n\t"
  "v_smfmac_f32_32x32x32_bf16 v[0:15], v[132:135], v[24:31], v152\n\t"
  "v_exp_f32_e32 v34, v34\n\t"
  "v_exp_f32_e32 v35, v35\n\t"
  "v_exp_f32_e32 v36, v36\n\t"
  "s_waitcnt vmcnt(0)\n\t"
  "v_mfma_f32_32x32x16_bf16 v[16:31], v[48:51], v[144:147], 0\n\t"
  "v_exp_f32_e32 v37, v37\n\t"
  "v_exp_f32_e32 v38, v38\n\t"
  "ds_read_b32 v155, v162\n\t"
  "v_exp_f32_e32 v39, v39\n\t"
  "v_exp_f32_e32 v40, v40\n\t"
  "v_exp_f32_e32 v41, v41\n\t"
  "v_smfmac_f32_32x32x32_bf16 v[0:15], v[136:139], v[32:39], v152\n\t"
  "v_exp_f32_e32 v42, v42\n\t"
  "v_exp_f32_e32 v43, v43\n\t"
  "v_exp_f32_e32 v44, v44\n\t"
  "v_exp_f32_e32 v45, v45\n\t"
  "v_exp_f32_e32 v46, v46\n\t"
  "v_exp_f32_e32 v47, v47\n\t"
  "s_add_u32 s74, s74, 1\n\t"
  "s_branch Lloop_%=\n\t"
  "Llast_%=:\n\t"
  "v_exp_f32_e32 v16, v16\n\t"
  "v_exp_f32_e32 v17, v17\n\t"
  "v_smfmac_f32_32x32x32_bf16 v[0:15], v[140:143], v[40:47], v152\n\t"
  "v_exp_f32_e32 v18, v18\n\t"
  "v_exp_f32_e32 v19, v19\n\t"
  "v_exp_f32_e32 v20, v20\n\t"
  "v_mfma_f32_32x32x16_bf16 v[32:47], v[52:55], v[148:151], 0\n\t"
  "v_exp_f32_e32 v21, v21\n\t"
  "v_exp_f32_e32 v22, v22\n\t"
  "v_exp_f32_e32 v23, v23\n\t"
  "v_exp_f32_e32 v24, v24\n\t"
  "v_exp_f32_e32 v25, v25\n\t"
  "v_exp_f32_e32 v26, v26\n\t"
  "v_exp_f32_e32 v27, v27\n\t"
  "v_exp_f32_e32 v28, v28\n\t"
  "ds_write_b128 v160, v[0:3]\n\t"
  "ds_write_b128 v160, v[4:7] offset:1024\n\t"
  "ds_write_b32 v161, v8\n\t"
  "s_mov_b64 exec, 1\n\t"
  "ds_write_b32 v162, v159\n\t"
  "s_mov_b64 exec, -1\n\t"
  "s_mul_i32 s71, s70, 2304\n\t"
  "v_add_u32_e32 v160, s71, v153\n\t"
  "v_add_u32_e32 v161, s71, v154\n\t"
  "s_waitcnt lgkmcnt(4)\n\t"
  "v_readfirstlane_b32 s75, v155\n\t"
  "s_cmp_ge_u32 s75, s74\n\t"
  "s_cbranch_scc1 Lrdy3_%=\n\t"
  "Lspin3_%=:\n\t"
  "s_sleep 2\n\t"
  "ds_read_b32 v155, v163\n\t"
  "s_waitcnt lgkmcnt(0)\n\t"
  "v_readfirstlane_b32 s75, v155\n\t"
  "s_cmp_ge_u32 s75, s74\n\t"
  "s_cbranch_scc0 Lspin3_%=\n\t"
  "Lrdy3_%=:\n\t"
  "ds_read_b128 v[0:3], v160\n\t"
  "ds_read_b128 v[4:7], v160 offset:1024\n\t"
  "ds_read_b32 v8, v161\n\t"
  "v_exp_f32_e32 v29, v29\n\t"
  "v_exp_f32_e32 v30, v30\n\t"
  "v_exp_f32_e32 v31, v31\n\t"
  "v_exp_f32_e32 v32, v32\n\t"
  "s_waitcnt lgkmcnt(0)\n\t"
  "v_smfmac_f32_32x32x32_bf16 v[0:15], v[80:83], v[16:23], v152\n\t"
  "v_exp_f32_e32 v33, v33\n\t"
  "v_exp_f32_e32 v34, v34\n\t"
  "v_smfmac_f32_32x32x32_bf16 v[0:15], v[84:87], v[24:31], v152\n\t"
  "v_exp_f32_e32 v35, v35\n\t"
  "v_exp_f32_e32 v36, v36\n\t"
  "v_mfma_f32_32x32x16_bf16 v[16:31], v[56:59], v[148:151], 0\n\t"
  "v_exp_f32_e32 v37, v37\n\t"
  "v_exp_f32_e32 v38, v38\n\t"
  "v_exp_f32_e32 v39, v39\n\t"
  "v_exp_f32_e32 v40, v40\n\t"
  "v_exp_f32_e32 v41, v41\n\t"
  "v_smfmac_f32_32x32x32_bf16 v[0:15], v[88:91], v[32:39], v152\n\t"
  "v_exp_f32_e32 v42, v42\n\t"
  "v_exp_f32_e32 v43, v43\n\t"
  "v_exp_f32_e32 v44, v44\n\t"
  "v_exp_f32_e32 v45, v45\n\t"
  "v_exp_f32_e32 v46, v46\n\t"
  "v_exp_f32_e32 v47, v47\n\t"
  "v_exp_f32_e32 v16, v16\n\t"
  "v_exp_f32_e32 v17, v17\n\t"
  "v_smfmac_f32_32x32x32_bf16 v[0:15], v[92:95], v[40:47], v152\n\t"
  "v_exp_f32_e32 v18, v18\n\t"
  "v_exp_f32_e32 v19, v19\n\t"
  "v_exp_f32_e32 v20, v20\n\t"
  "v_mfma_f32_32x32x16_bf16 v[32:47], v[60:63], v[148:151], 0\n\t"
  "v_exp_f32_e32 v21, v21\n\t"
  "v_exp_f32_e32 v22, v22\n\t"
  "v_exp_f32_e32 v23, v23\n\t"
  "v_exp_f32_e32 v24, v24\n\t"
  "v_exp_f32_e32 v25, v25\n\t"
  "v_smfmac_f32_32x32x32_bf16 v[0:15], v[96:99], v[16:23], v152\n\t"
  "v_exp_f32_e32 v26, v26\n\t"
  "v_exp_f32_e32 v27, v27\n\t"
  "v_exp_f32_e32 v28, v28\n\t"
  "v_exp_f32_e32 v29, v29\n\t"
  "v_exp_f32_e32 v30, v30\n\t"
  "v_exp_f32_e32 v31, v31\n\t"
  "v_exp_f32_e32 v32, v32\n\t"
  "v_exp_f32_e32 v33, v33\n\t"
  "v_smfmac_f32_32x32x32_bf16 v[0:15], v[100:103], v[24:31], v152\n\t"
  "v_exp_f32_e32 v34, v34\n\t"
  "v_exp_f32_e32 v35, v35\n\t"
  "v_exp_f32_e32 v36, v36\n\t"
  "v_mfma_f32_32x32x16_bf16 v[16:31], v[64:67], v[148:151], 0\n\t"
  "v_exp_f32_e32 v37, v37\n\t"
  "v_exp_f32_e32 v38, v38\n\t"
  "v_exp_f32_e32 v39, v39\n\t"
  "v_exp_f32_e32 v40, v40\n\t"
  "v_exp_f32_e32 v41, v41\n\t"
  "v_smfmac_f32_32x32x32_bf16 v[0:15], v[104:107], v[32:39], v152\n\t"
  "v_exp_f32_e32 v42, v42\n\t"
  "v_exp_f32_e32 v43, v43\n\t"
  "v_exp_f32_e32 v44, v44\n\t"
  "v_exp_f32_e32 v45, v45\n\t"
  "v_exp_f32_e32 v46, v46\n\t"
  "v_exp_f32_e32 v47, v47\n\t"
  "v_exp_f32_e32 v16, v16\n\t"
  "v_exp_f32_e32 v17, v17\n\t"
  "v_smfmac_f32_32x32x32_bf16 v[0:15], v[108:111], v[40:47], v152\n\t"
  "v_exp_f32_e32 v18, v18\n\t"
  "v_exp_f32_e32 v19, v19\n\t"
  "v_exp_f32_e32 v20, v20\n\t"
  "v_mfma_f32_32x32x16_bf16 v[32:47], v[68:71], v[148:151], 0\n\t"
  "v_exp_f32_e32 v21, v21\n\t"
  "v_exp_f32_e32 v22, v22\n\t"
  "v_exp_f32_e32 v23, v23\n\t"
  "v_exp_f32_e32 v24, v24\n\t"
  "v_exp_f32_e32 v25, v25\n\t"
  "v_smfmac_f32_32x32x32_bf16 v[0:15], v[112:115], v[16:23], v152\n\t"
  "v_exp_f32_e32 v26, v26\n\t"
  "v_exp_f32_e32 v27, v27\n\t"
  "v_exp_f32_e32 v28, v28\n\t"
  "v_exp_f32_e32 v29, v29\n\t"
  "v_exp_f32_e32 v30, v30\n\t"
  "v_exp_f32_e32 v31, v31\n\t"
  "v_exp_f32_e32 v32, v32\n\t"
  "v_exp_f32_e32 v33, v33\n\t"
  "v_smfmac_f32_32x32x32_bf16 v[0:15], v[116:119], v[24:31], v152\n\t"
  "v_exp_f32_e32 v34, v34\n\t"
  "v_exp_f32_e32 v35, v35\n\t"
  "v_exp_f32_e32 v36, v36\n\t"
  "v_mfma_f32_32x32x16_bf16 v[16:31], v[72:75], v[148:151], 0\n\t"
  "v_exp_f32_e32 v37, v37\n\t"
  "v_exp_f32_e32 v38, v38\n\t"
  "v_exp_f32_e32 v39, v39\n\t"
  "v_exp_f32_e32 v40, v40\n\t"
  "v_exp_f32_e32 v41, v41\n\t"
  "v_smfmac_f32_32x32x32_bf16 v[0:15], v[120:123], v[32:39], v152\n\t"
  "v_exp_f32_e32 v42, v42\n\t"
  "v_exp_f32_e32 v43, v43\n\t"
  "v_exp_f32_e32 v44, v44\n\t"
  "v_exp_f32_e32 v45, v45\n\t"
  "v_exp_f32_e32 v46, v46\n\t"
  "v_exp_f32_e32 v47, v47\n\t"
  "v_exp_f32_e32 v16, v16\n\t"
  "v_exp_f32_e32 v17, v17\n\t"
  "v_smfmac_f32_32x32x32_bf16 v[0:15], v[124:127], v[40:47], v152\n\t"
  "v_exp_f32_e32 v18, v18\n\t"
  "v_exp_f32_e32 v19, v19\n\t"
  "v_exp_f32_e32 v20, v20\n\t"
  "v_mfma_f32_32x32x16_bf16 v[32:47], v[76:79], v[148:151], 0\n\t"
  "v_exp_f32_e32 v21, v21\n\t"
  "v_exp_f32_e32 v22, v22\n\t"
  "v_exp_f32_e32 v23, v23\n\t"
  "v_exp_f32_e32 v24, v24\n\t"
  "v_exp_f32_e32 v25, v25\n\t"
  "v_smfmac_f32_32x32x32_bf16 v[0:15], v[128:131], v[16:23], v152\n\t"
  "v_exp_f32_e32 v26, v26\n\t"
  "v_exp_f32_e32 v27, v27\n\t"
  "v_exp_f32_e32 v28, v28\n\t"
  "v_exp_f32_e32 v29, v29\n\t"
  "v_exp_f32_e32 v30, v30\n\t"
  "v_exp_f32_e32 v31, v31\n\t"
  "v_exp_f32_e32 v32, v32\n\t"
  "v_exp_f32_e32 v33, v33\n\t"
  "v_smfmac_f32_32x32x32_bf16 v[0:15], v[132:135], v[24:31], v152\n\t"
  "v_exp_f32_e32 v34, v34\n\t"
  "v_exp_f32_e32 v35, v35\n\t"
  "v_exp_f32_e32 v36, v36\n\t"
  "v_exp_f32_e32 v37, v37\n\t"
  "v_exp_f32_e32 v38, v38\n\t"
  "v_exp_f32_e32 v39, v39\n\t"
  "v_exp_f32_e32 v40, v40\n\t"
  "v_exp_f32_e32 v41, v41\n\t"
  "v_smfmac_f32_32x32x32_bf16 v[0:15], v[136:139], v[32:39], v152\n\t"
  "v_exp_f32_e32 v42, v42\n\t"
  "v_exp_f32_e32 v43, v43\n\t"
  "v_exp_f32_e32 v44, v44\n\t"
  "v_exp_f32_e32 v45, v45\n\t"
  "v_exp_f32_e32 v46, v46\n\t"
  "v_exp_f32_e32 v47, v47\n\t"
  "s_nop 1\n\t"
  "v_smfmac_f32_32x32x32_bf16 v[0:15], v[140:143], v[40:47], v152\n\t"
  "s_nop 12\n\t"
  "ds_write_b128 v160, v[0:3]\n\t"
  "ds_write_b128 v160, v[4:7] offset:1024\n\t"
  "ds_write_b32 v161, v8\n\t"
  "s_mov_b64 exec, 1\n\t"
  "ds_write_b32 v163, v159\n\t"
  "s_mov_b64 exec, -1\n\t"
  "s_waitcnt lgkmcnt(0)\n\t"
    :
    : [ov] "v"(ov), "{v152}"(0xDDDDu), "{v153}"(ring + lane * 16), "{v154}"(ring + 2048 + lane * 4), "{v156}"(AT_FLAG + (w >> 2) * 32),
      "{v157}"(lane * 16), "{v158}"(((lane & 15) + ((lane >> 5) << 4)) * 16),
      [kb] "s"(kb), [vb] "s"(vb), [qlo] "s"((u32)qa), [qhi] "s"((u32)(qa >> 32)), [p2] "s"(2 * (w & 3))
    : "memory", "scc", "s70", "s71", "s72", "s73", "s74", "s75", "s76", "s77",
      "v0","v1","v2","v3","v4","v5","v6","v7","v8","v9","v10","v11","v12","v13","v14","v15","v16","v17","v18","v19",
      "v20","v21","v22","v23","v24","v25","v26","v27","v28","v29","v30","v31","v32","v33","v34","v35","v36","v37","v38","v39",
      "v40","v41","v42","v43","v44","v45","v46","v47","v48","v49","v50","v51","v52","v53","v54","v55","v56","v57","v58","v59",
      "v60","v61","v62","v63","v64","v65","v66","v67","v68","v69","v70","v71","v72","v73","v74","v75","v76","v77","v78","v79",
      "v80","v81","v82","v83","v84","v85","v86","v87","v88","v89","v90","v91","v92","v93","v94","v95","v96","v97","v98","v99","v100","v101","v102","v103","v104","v105","v106","v107","v108","v109","v110","v111","v112","v113","v114","v115","v116","v117","v118","v119","v120","v121","v122","v123","v124","v125","v126","v127","v128","v129","v130","v131","v132","v133","v134","v135","v136","v137","v138","v139","v140","v141","v142","v143",
      "v144","v145","v146","v147","v148","v149","v150","v151","v155","v159","v160","v161","v162","v163");
}
DEV u32 lds_poll(u32 a) { return __builtin_amdgcn_readfirstlane(*(volatile LAS u32*)(size_t)a); }
DEV void attn_slow_tile(const Params& p, int head, int qtile, int lane, float* o) {
  u32x4 q; ld16_async(q, p.ws + WS_Q, ((head * 64 + qtile) * 64 + lane) * 16);
  const u32 ov = ((lane & 27) == 16) ? 0x3f803f80u : 0u;
  const u32 vo = ((lane & 15) + ((lane >> 5) << 4)) * 16;
  f32x16 O = {0}; float m = -3.0e38f;
  for (int t = 0; t < 64; t++) {
    u32x4 k, v0, v1;
    ld16_async(k, p.ws + WS_K, ((head * 64 + t) * 64 + lane) * 16);
    ld16_async(v0, p.ws + WS_V, (head * 64 + t) * 1024 + vo);
    ld16_async(v1, p.ws + WS_V, (head * 64 + t) * 1024 + 512 + vo);
    asm volatile("s_waitcnt vmcnt(0)" : "+v"(q), "+v"(k), "+v"(v0), "+v"(v1) :: "memory");
    if (lane & 16) { v0 = u32x4{ov, ov, ov, ov}; v1 = v0; }
    f32x16 z = {0};
    f32x16 s = mfma32(k, q, z);
    float tm = s[0];
    for (int i = 1; i < 16; i++) tm = fmaxf(tm, s[i]);
    tm = fmaxf(tm, xhalf(tm));
    float mn = fmaxf(m, tm), al = __builtin_amdgcn_exp2f(m - mn);
    m = mn;
    for (int i = 0; i < 9; i++) O[i] *= al;
    for (int i = 0; i < 16; i++) s[i] = __builtin_amdgcn_exp2f(s[i] - m);
    typedef float f32x8 __attribute__((ext_vector_type(8)));
    typedef __bf16 bf16x16 __attribute__((ext_vector_type(16)));
    f32x8 lo = __builtin_shufflevector(s, s, 0, 1, 2, 3, 4, 5, 6, 7), hi = __builtin_shufflevector(s, s, 8, 9, 10, 11, 12, 13, 14, 15);
    O = __builtin_amdgcn_smfmac_f32_32x32x32_bf16(__builtin_bit_cast(bf16x8, v0), __builtin_bit_cast(bf16x16, lo), O, 0xDDDD, 0, 0);
    O = __builtin_amdgcn_smfmac_f32_32x32x32_bf16(__builtin_bit_cast(bf16x8, v1), __builtin_bit_cast(bf16x16, hi), O, 0xDDDD, 0, 0);
  }
  for (int i = 0; i < 9; i++) o[i] = O[i];
}
DEV void attn_finalize(const Params& p, int head, int qb, int w, int lane) {
  u32 nk, nq, g0, g1;
  ld4_async(nk, p.ws + WS_NK, (head * 64 + lane) * 4);
  ld4_async(nq, p.ws + WS_NQ, (head * 64 + qb * 8 + (lane & 7)) * 4);
  ld4_async(g0, p.ws + WS_G, (qb * 256 + w * 64 + (lane & 31)) * 4);
  ld4_async(g1, p.ws + WS_G, (qb * 256 + w * 64 + 32 + (lane & 31)) * 4);
  asm volatile("s_waitcnt vmcnt(0)" : "+v"(nk), "+v"(nq), "+v"(g0), "+v"(g1) :: "memory");
  const float bound2 = wave_max(__uint_as_float(nk)) * wave_max(__uint_as_float(nq));
  const bool safe = bound2 <= SAFE_BOUND2;
  #pragma unroll
  for (int j = 0; j < 2; j++) {
    const int qt = 2 * w + j;
    float o[9];
    if (safe) {
      while (lds_poll(AT_FLAG + qt * 4) < 4 || lds_poll(AT_FLAG + 32 + qt * 4) < 4) __builtin_amdgcn_s_sleep(4);
      u32 a = AT_RING + qt * AT_SLOT, b = a + 8 * AT_SLOT;
      f32x4 a0 = *(const LAS f32x4*)(size_t)(a + lane * 16), a1 = *(const LAS f32x4*)(size_t)(a + 1024 + lane * 16);
      f32x4 b0 = *(const LAS f32x4*)(size_t)(b + lane * 16), b1 = *(const LAS f32x4*)(size_t)(b + 1024 + lane * 16);
      float a2 = *(const LAS float*)(size_t)(a + 2048 + lane * 4), b2 = *(const LAS float*)(size_t)(b + 2048 + lane * 4);
      for (int i = 0; i < 4; i++) { o[i] = a0[i] + b0[i]; o[4 + i] = a1[i] + b1[i]; }
      o[8] = a2 + b2;
    } else {
      attn_slow_tile(p, head, qb * 8 + qt, lane, o);
    }
    float sc = __uint_as_float(j ? g1 : g0) * __builtin_amdgcn_rcpf(o[8]);
    u32x4 r = {pack2(o[0] * sc, o[1] * sc), pack2(o[2] * sc, o[3] * sc), pack2(o[4] * sc, o[5] * sc), pack2(o[6] * sc, o[7] * sc)};
    st16_wt(p.ws + WS_O, (((qb * 8 + qt) * 32 + head) * 64 + lane) * 16, r);
  }
}

DEV void garrive(u32 cnt, int lane) { if (lane == 0) __hip_atomic_fetch_add((LAS u32*)(size_t)cnt, 1u, __ATOMIC_RELAXED, __HIP_MEMORY_SCOPE_WORKGROUP); }
DEV void gwait(u32 cnt, u32 target) { while (lds_poll(cnt) < target) __builtin_amdgcn_s_sleep(1); }

DEV void gemm1(const Params& p, int dt, int ft, int ks, int w, int lane, u32 LW, u32 cnt) {
  u32 off[3];
  for (int j = 0; j < 3; j++) {
    int e = (w * 3 + j) * 64 + lane, r = e / 12, c = e - r * 12;
    if (ft == 14) c &= 3;
    off[j] = r * 2752 + c * 16;
  }
  const char* wb = (const char*)p.w1 + (size_t)ks * 512 * 2752 + ft * 192;
  for (int s = 0; s < 8; s++)
    for (int j = 0; j < 3; j++) glds16(wb + s * 64 * 2752, off[j], LW + s * 12288 + (w * 3 + j) * 1024);
  const int c31 = lane & 31, h = lane >> 5;
  const char* hb = p.ws + WS_H + (size_t)ks * 64 * 8192;
  const u32 hoff = h * 8192 + (dt * 128 + w * 32 + c31) * 16;
  u32x4 hf[8];
  #pragma unroll
  for (int i = 0; i < 8; i++) ld16_async(hf[i], hb + i * 16384, hoff);
  u32 wa;
  { int g4 = (lane >> 4) & 1, i = lane & 15; wa = LW + (8 * h + (i >> 2)) * 192 + g4 * 32 + (i & 3) * 8; }
  f32x16 acc[3] = {};
  VMWAIT(8);
  garrive(cnt, lane); gwait(cnt, 4);
  #pragma unroll
  for (int k = 0; k < 32; k++) {
    if (k < 25) asm volatile("s_waitcnt vmcnt(7)" : "+v"(hf[k & 7]) :: "memory");
    else if (k == 25) asm volatile("s_waitcnt vmcnt(6)" : "+v"(hf[k & 7]) :: "memory");
    else if (k == 26) asm volatile("s_waitcnt vmcnt(5)" : "+v"(hf[k & 7]) :: "memory");
    else if (k == 27) asm volatile("s_waitcnt vmcnt(4)" : "+v"(hf[k & 7]) :: "memory");
    else if (k == 28) asm volatile("s_waitcnt vmcnt(3)" : "+v"(hf[k & 7]) :: "memory");
    else if (k == 29) asm volatile("s_waitcnt vmcnt(2)" : "+v"(hf[k & 7]) :: "memory");
    else if (k == 30) asm volatile("s_waitcnt vmcnt(1)" : "+v"(hf[k & 7]) :: "memory");
    else asm volatile("s_waitcnt vmcnt(0)" : "+v"(hf[k & 7]) :: "memory");
    #pragma unroll
    for (int j = 0; j < 3; j++) {
      u32x4 wf = lds_tr2(wa + k * 3072 + j * 64, wa + k * 3072 + j * 64 + 768);
      acc[j] = mfma32(wf, hf[k & 7], acc[j]);
    }
    if (k + 8 < 32) { ld16_async(hf[k & 7], hb + (k + 8) * 16384, hoff); }
  }
  char* pb = p.ws + WS_P + (size_t)(((dt * 15 + ft) * 4 + ks) * 4 + w) * 12288;
  #pragma unroll
  for (int j = 0; j < 3; j++)
    if (ft < 14 || j == 0)
      #pragma unroll
      for (int q = 0; q < 4; q++) {
        u32x4 v = {__float_as_uint(acc[j][4 * q]), __float_as_uint(acc[j][4 * q + 1]), __float_as_uint(acc[j][4 * q + 2]), __float_as_uint(acc[j][4 * q + 3])};
        st16_wt(pb, (j * 4 + q) * 1024 + lane * 16, v);
      }
}
DEV float gelu_t(float a) {
  float z = a * (1.0f + 0.044715f * a * a) * (-2.0f * 0.7978845608028654f * 1.4426950408889634f);
  return a * __builtin_amdgcn_rcpf(1.0f + __builtin_amdgcn_exp2f(z));
}
DEV void gemm1_reduce(const Params& p, int dt, int ft, int ks, int j, int lane) {
  if (j >= 3 || (ft == 14 && j > 0)) return;
  const char* pb = p.ws + WS_P + (size_t)((dt * 15 + ft) * 16 + ks) * 12288 + j * 4096;
  u32x4 v[4][4];
  #pragma unroll
  for (int s = 0; s < 4; s++)
    #pragma unroll
    for (int q = 0; q < 4; q++) ld16_async(v[s][q], pb + s * 4 * 12288, q * 1024 + lane * 16);
  const int h = lane >> 5, f0 = ft * 96 + j * 32;
  u32x2 bb[4];
  for (int q = 0; q < 4; q++) bb[q] = *(const u32x2*)(p.b1 + f0 + 8 * q + 4 * h);
  asm volatile("s_waitcnt vmcnt(0)" : "+v"(v[0][0]), "+v"(v[0][1]), "+v"(v[0][2]), "+v"(v[0][3]), "+v"(v[1][0]), "+v"(v[1][1]), "+v"(v[1][2]), "+v"(v[1][3]),
               "+v"(v[2][0]), "+v"(v[2][1]), "+v"(v[2][2]), "+v"(v[2][3]), "+v"(v[3][0]), "+v"(v[3][1]), "+v"(v[3][2]), "+v"(v[3][3]) :: "memory");
  u32 o[8];
  #pragma unroll
  for (int q = 0; q < 4; q++) {
    float r[4];
    for (int i = 0; i < 4; i++) {
      float a = (__uint_as_float(v[0][q][i]) + __uint_as_float(v[1][q][i])) + (__uint_as_float(v[2][q][i]) + __uint_as_float(v[3][q][i]));
      a += (i & 1) ? bf_hi(bb[q][i >> 1]) : bf_lo(bb[q][i >> 1]);
      r[i] = gelu_t(a);
    }
    o[2 * q] = pack2(r[0], r[1]); o[2 * q + 1] = pack2(r[2], r[3]);
  }
  const u32 uo = (((f0 >> 4) * 2 + h) * 512 + dt * 128 + ks * 32 + (lane & 31)) * 16;
  st16_wt(p.ws + WS_U, uo, u32x4{o[0], o[1], o[2], o[3]});
  st16_wt(p.ws + WS_U, uo + 16384, u32x4{o[4], o[5], o[6], o[7]});
}

constexpr int G2_RING = 12;
DEV void gemm2_out(const Params& p, int st, int dt, int w, int lane, u32 LW, u32 cnt, u32 cnt0) {
  const int c31 = lane & 31, h = lane >> 5;
  const int k0 = (w < 2) ? 22 * w : 44 + 21 * (w - 2), nk = (w < 2) ? 22 : 21;
  const u32 ring = LW + w * G2_RING * 2048;
  u32 wv;
  { int r = lane >> 3; wv = ((lane & 7) ^ (((r >> 1) & 1) << 2)) << 4; wv |= r << 12; }
  const char* w2b = (const char*)p.w2 + (size_t)k0 * 16 * 4096 + st * 128;
  auto issue = [&](int k) { u32 d = ring + (k % G2_RING) * 2048; const char* b = w2b + (size_t)k * 65536; glds16(b, wv, d); glds16(b + 32768, wv, d + 1024); };
  const char* ub = p.ws + WS_U + (size_t)k0 * 16384;
  const u32 uoff = h * 8192 + (dt * 64 + c31) * 16;
  u32 ta[2];
  { int g4 = (lane >> 4) & 1, i = lane & 15, r = 4 * h + (i >> 2);
    for (int jc = 0; jc < 2; jc++) ta[jc] = r * 128 + (((4 * jc + 2 * g4 + ((i & 3) >> 1)) ^ (((r >> 1) & 1) << 2)) << 4) + (i & 1) * 8; }
  constexpr int PD = 4;
  u32x4 uf[PD][2];
  #pragma unroll
  for (int k = 0; k < PD; k++) { issue(k); ld16_async(uf[k][0], ub + k * 16384, uoff); ld16_async(uf[k][1], ub + k * 16384 + 512, uoff); }
  f32x16 acc[2][2] = {};
  #pragma unroll
  for (int k = 0; k < 22; k++) {
    if (k < 21 || nk == 22) {
      if (k + PD <= 21) asm volatile("s_waitcnt vmcnt(12)" : "+v"(uf[k % PD][0]), "+v"(uf[k % PD][1]) :: "memory");
      else asm volatile("s_waitcnt vmcnt(0)" : "+v"(uf[k % PD][0]), "+v"(uf[k % PD][1]) :: "memory");
      u32 s = ring + (k % G2_RING) * 2048;
      u32x4 b0 = lds_tr2(s + ta[0], s + ta[0] + 1024), b1 = lds_tr2(s + ta[1], s + ta[1] + 1024);
      acc[0][0] = mfma32(uf[k % PD][0], b0, acc[0][0]); acc[0][1] = mfma32(uf[k % PD][0], b1, acc[0][1]);
      acc[1][0] = mfma32(uf[k % PD][1], b0, acc[1][0]); acc[1][1] = mfma32(uf[k % PD][1], b1, acc[1][1]);
      if (k + PD < 21 || (k + PD == 21 && nk == 22)) {
        issue(k + PD); ld16_async(uf[k % PD][0], ub + (k + PD) * 16384, uoff); ld16_async(uf[k % PD][1], ub + (k + PD) * 16384 + 512, uoff);
      }
    }
  }
  LGKMWAIT(0);
  {
    u32 wv2 = (wv & 0xfff) | ((lane >> 3) << 10);
    const char* wob = (const char*)p.wo + (size_t)w * 128 * 1024 + dt * 128;
    for (int i = 0; i < 16; i++) glds16(wob + i * 8192, wv2, ring + i * 1024);
  }
  u32 g0, g1;
  ld4_async(g0, p.ws + WS_G, (st * 64 + c31) * 4); ld4_async(g1, p.ws + WS_G, (st * 64 + 32 + c31) * 4);
  u32x4 of[8][2];
  #pragma unroll
  for (int k = 0; k < 8; k++) {
    ld16_async(of[k][0], p.ws + WS_O + (size_t)((st * 2) * 32 + w * 8 + k) * 1024, lane * 16);
    ld16_async(of[k][1], p.ws + WS_O + (size_t)((st * 2 + 1) * 32 + w * 8 + k) * 1024, lane * 16);
  }
  float ba = 0.f, bb = 0.f;
  if (w == 0) { ba = bf_lo(p.b2[st * 64 + c31]); bb = bf_lo(p.b2[st * 64 + 32 + c31]); }
  asm volatile("s_waitcnt vmcnt(0)" : "+v"(g0), "+v"(g1), "+v"(of[0][0]), "+v"(of[0][1]), "+v"(of[1][0]), "+v"(of[1][1]), "+v"(of[2][0]), "+v"(of[2][1]), "+v"(of[3][0]), "+v"(of[3][1]),
               "+v"(of[4][0]), "+v"(of[4][1]), "+v"(of[5][0]), "+v"(of[5][1]), "+v"(of[6][0]), "+v"(of[6][1]), "+v"(of[7][0]), "+v"(of[7][1]) :: "memory");
  const float s0 = 1.0f - __uint_as_float(g0), s1 = 1.0f - __uint_as_float(g1);
  for (int jd = 0; jd < 2; jd++) for (int i = 0; i < 16; i++) { acc[jd][0][i] = (acc[jd][0][i] + ba) * s0; acc[jd][1][i] = (acc[jd][1][i] + bb) * s1; }
  #pragma unroll
  for (int k = 0; k < 8; k++) {
    u32 s = ring + k * 2048;
    u32x4 a0 = lds_tr2(s + ta[0], s + ta[0] + 1024), a1 = lds_tr2(s + ta[1], s + ta[1] + 1024);
    acc[0][0] = mfma32(a0, of[k][0], acc[0][0]); acc[0][1] = mfma32(a0, of[k][1], acc[0][1]);
    acc[1][0] = mfma32(a1, of[k][0], acc[1][0]); acc[1][1] = mfma32(a1, of[k][1], acc[1][1]);
  }
  LGKMWAIT(0);
  for (int jd = 0; jd < 2; jd++) for (int js = 0; js < 2; js++) for (int q = 0; q < 4; q++) {
    f32x4 v = {acc[jd][js][4 * q], acc[jd][js][4 * q + 1], acc[jd][js][4 * q + 2], acc[jd][js][4 * q + 3]};
    *(LAS f32x4*)(size_t)(ring + (js * 32 + c31) * 272 + (jd * 32 + 8 * q + 4 * h) * 4) = v;
  }
  LGKMWAIT(0);
  garrive(cnt, lane); gwait(cnt, cnt0 + 4);
  {
    const int tok = w * 16 + (lane >> 2), d0 = (lane & 3) * 16;
    f32x4 r[4];
    for (int q = 0; q < 4; q++) {
      u32 a = LW + tok * 272 + (d0 + 4 * q) * 4;
      r[q] = (*(const LAS f32x4*)(size_t)a + *(const LAS f32x4*)(size_t)(a + G2_RING * 2048)) + (*(const LAS f32x4*)(size_t)(a + 2 * G2_RING * 2048) + *(const LAS f32x4*)(size_t)(a + 3 * G2_RING * 2048));
    }
    u32x4 o0 = {pack2(r[0][0], r[0][1]), pack2(r[0][2], r[0][3]), pack2(r[1][0], r[1][1]), pack2(r[1][2], r[1][3])};
    u32x4 o1 = {pack2(r[2][0], r[2][1]), pack2(r[2][2], r[2][3]), pack2(r[3][0], r[3][1]), pack2(r[3][2], r[3][3])};
    u32x4* dst = (u32x4*)(p.out + (size_t)(st * 64 + tok) * 512 + dt * 64 + d0);
    dst[0] = o0; dst[1] = o1;
  }
}

__global__ void __launch_bounds__(768, 3) k_phase1(Params p) {
  const int w = __builtin_amdgcn_readfirstlane(threadIdx.x >> 6), lane = threadIdx.x & 63;
  phase1(p, blockIdx.x >> 3, blockIdx.x & 7, w, lane);
}
__global__ void __launch_bounds__(768, 3) k_attn(Params p) {
  const int w = __builtin_amdgcn_readfirstlane(threadIdx.x >> 6), lane = threadIdx.x & 63;
  if (threadIdx.x < 16) *(LAS u32*)(size_t)(AT_FLAG + threadIdx.x * 4) = 0;
  __syncthreads();
  const int xcd = blockIdx.x & 7, i = blockIdx.x >> 3, head = xcd * 4 + (i >> 3), qb = i & 7;
  if (w < 8) {
    attn_wave(p, head, qb, w, lane);
    if (w < 4) attn_finalize(p, head, qb, w, lane);
  }
}
__global__ void __launch_bounds__(256, 3) k_g1(Params p) {
  const int w = __builtin_amdgcn_readfirstlane(threadIdx.x >> 6), lane = threadIdx.x & 63;
  if (threadIdx.x == 0) *(LAS u32*)(size_t)98304 = 0;
  __syncthreads();
  const int b = blockIdx.x, ks = b & 3, t = b >> 2;
  gemm1(p, t / 15, t % 15, ks, w, lane, 0, 98304);
}
__global__ void __launch_bounds__(256) k_red(Params p) {
  const int w = __builtin_amdgcn_readfirstlane(threadIdx.x >> 6), lane = threadIdx.x & 63;
  const int b = blockIdx.x, ks = b & 3, t = b >> 2;
  gemm1_reduce(p, t / 15, t % 15, ks, w, lane);
}
__global__ void __launch_bounds__(256, 2) k_fin(Params p) {
  const int w = __builtin_amdgcn_readfirstlane(threadIdx.x >> 6), lane = threadIdx.x & 63;
  if (threadIdx.x == 0) *(LAS u32*)(size_t)98304 = 0;
  __syncthreads();
  const int xcd = blockIdx.x & 7, i = blockIdx.x >> 3;
  gemm2_out(p, xcd * 4 + (i >> 3), i & 7, w, lane, 0, 98304, 0);
}

extern "C" void kernel_launch(void* const* d_in, const int* in_sizes, int n_in, void* d_out, int out_size, void* d_ws, size_t ws_size, hipStream_t stream) {
  Params p;
  p.x = (const u16*)d_in[0]; p.wqkv = (const u16*)d_in[1]; p.wo = (const u16*)d_in[2];
  p.w1 = (const u16*)d_in[4]; p.b1 = (const u16*)d_in[5]; p.w2 = (const u16*)d_in[6]; p.b2 = (const u16*)d_in[7]; p.gw = (const u16*)d_in[8];
  p.out = (u16*)d_out; p.ws = (char*)d_ws;
  (void)hipFuncSetAttribute((const void*)k_phase1, hipFuncAttributeMaxDynamicSharedMemorySize, 163840);
  (void)hipFuncSetAttribute((const void*)k_g1, hipFuncAttributeMaxDynamicSharedMemorySize, 163840);
  (void)hipFuncSetAttribute((const void*)k_fin, hipFuncAttributeMaxDynamicSharedMemorySize, 163840);
  k_phase1<<<256, 768, 147456, stream>>>(p);
  k_attn<<<256, 768, 40960, stream>>>(p);
  k_g1<<<240, 256, 98320, stream>>>(p);
  k_red<<<240, 256, 0, stream>>>(p);
  k_fin<<<256, 256, 98320, stream>>>(p);
}
```

```cpp
#include <hip/hip_runtime.h>
#include <hip/hip_bf16.h>
#include <type_traits>

typedef unsigned short u16;
typedef unsigned int u32;
typedef float f32x4 __attribute__((ext_vector_type(4)));
typedef float f32x16 __attribute__((ext_vector_type(16)));
typedef u32 u32x2 __attribute__((ext_vector_type(2)));
typedef u32 u32x4 __attribute__((ext_vector_type(4)));
typedef __bf16 bf16x8 __attribute__((ext_vector_type(8)));
typedef __bf16 bf16x2 __attribute__((ext_vector_type(2)));
typedef short s16x4 __attribute__((ext_vector_type(4)));
#define LAS __attribute__((address_space(3)))
#define DEV __device__ __forceinline__

constexpr int S_ = 2048, D_ = 512;
constexpr size_t WS_Q = 0, WS_K = 2u << 20, WS_V = 4u << 20, WS_H = 6u << 20, WS_O = 8u << 20, WS_U = 10u << 20, WS_P = 12u << 20;
constexpr size_t WS_F1 = 18u << 20, WS_FP = WS_F1 + 32768, WS_FU = WS_FP + 131072, WS_EPOCH = WS_FU + 32768;
constexpr u32 EMPTY = 0xffffffffu;
constexpr float QSCALE = 0.25f * 1.4426950408889634f;
constexpr float SAFE_BOUND2 = 50.0f * 50.0f;

DEV float bf_lo(u32 v) { return __uint_as_float(v << 16); }
DEV float bf_hi(u32 v) { return __uint_as_float(v & 0xffff0000u); }
DEV u32 pack2(float a, float b) { bf16x2 t = {(__bf16)a, (__bf16)b}; return __builtin_bit_cast(u32, t); }
DEV f32x16 mfma32(u32x4 a, u32x4 b, f32x16 c) {
  return __builtin_amdgcn_mfma_f32_32x32x16_bf16(__builtin_bit_cast(bf16x8, a), __builtin_bit_cast(bf16x8, b), c, 0, 0, 0);
}
DEV void mfma32v(f32x16& c, u32x4 a, u32x4 b) { asm volatile("v_mfma_f32_32x32x16_bf16 %0, %1, %2, %0" : "+v"(c) : "v"(a), "v"(b)); }
DEV void glds16(const void* base, u32 voff, u32 ldsdst) {
  asm volatile("s_mov_b32 m0, %2\n\ts_nop 0\n\tglobal_load_lds_dwordx4 %0, %1" :: "v"(voff), "s"(base), "s"(ldsdst) : "memory");
}
#define VMWAIT(n) asm volatile("s_waitcnt vmcnt(" #n ")" ::: "memory")
#define LGKMWAIT(n) asm volatile("s_waitcnt lgkmcnt(" #n ")" ::: "memory")
DEV void raw_barrier() { asm volatile("s_waitcnt lgkmcnt(0)\n\ts_barrier" ::: "memory"); }
DEV u32x4 lds_r128(u32 a) { return *(const LAS u32x4*)(size_t)a; }
DEV u32x2 lds_tr(u32 a) { s16x4 t = __builtin_amdgcn_ds_read_tr16_b64_v4i16((LAS s16x4*)(size_t)a); return __builtin_bit_cast(u32x2, t); }
DEV u32x4 lds_tr2(u32 a0, u32 a1) { u32x2 x = lds_tr(a0), y = lds_tr(a1); u32x4 r = {x[0], x[1], y[0], y[1]}; return r; }
DEV __amdgpu_buffer_rsrc_t mkrsrc(void* base) { return __builtin_amdgcn_make_buffer_rsrc(base, 0, 0x02000000, 0x00020000); }
DEV void st16_wt(void* base, u32 voff, u32x4 v) { __builtin_amdgcn_raw_buffer_store_b128(v, mkrsrc(base), voff, 0, 16); }
DEV void ld16_async(u32x4& d, const void* base, u32 voff) {
  asm volatile("global_load_dwordx4 %0, %1, %2 sc1" : "=v"(d) : "v"(voff), "s"(base) : "memory");
}
DEV void ld4_async(u32& d, const void* base, u32 voff) {
  asm volatile("global_load_dword %0, %1, %2 sc1" : "=v"(d) : "v"(voff), "s"(base) : "memory");
}
DEV void warm(u32& d, const void* base, u32 voff) { asm volatile("global_load_dword %0, %1, %2" : "=v"(d) : "v"(voff), "s"(base) : "memory"); }
template <int CTRL> DEV float dppf(float v) { return __uint_as_float(__builtin_amdgcn_update_dpp(0, __float_as_int(v), CTRL, 0xf, 0xf, true)); }
DEV float row16_max(float m) {
  m = fmaxf(m, dppf<0xB1>(m)); m = fmaxf(m, dppf<0x4E>(m)); m = fmaxf(m, dppf<0x141>(m)); m = fmaxf(m, dppf<0x140>(m)); return m;
}
DEV float row16_sum(float m) {
  m += dppf<0xB1>(m); m += dppf<0x4E>(m); m += dppf<0x141>(m); m += dppf<0x140>(m); return m;
}
DEV float rdlane(float v, int l) { return __uint_as_float(__builtin_amdgcn_readlane(__float_as_int(v), l)); }
DEV float wave_sum(float m) { m = row16_sum(m); return (rdlane(m, 0) + rdlane(m, 16)) + (rdlane(m, 32) + rdlane(m, 48)); }
DEV int fresh_lane() { int l; asm volatile("v_mbcnt_lo_u32_b32 %0, -1, 0\n\tv_mbcnt_hi_u32_b32 %0, -1, %0" : "=v"(l)); return l; }
DEV float xhalf(float v, int lane) {
  u32 a = __float_as_uint(v);
  auto r = __builtin_amdgcn_permlane32_swap(a, a, false, false);
  return __uint_as_float((lane & 32) ? r[0] : r[1]);
}

typedef unsigned long long u64;
DEV bool unit_bad(const u32x4& f, u64 tag) { return f[1] != (u32)tag || f[3] != (u32)(tag >> 32); }
DEV u32x4 poll_flags(const char* ws, u32 voff, u64 tag) {
  u32x4 f;
  #pragma nounroll
  for (u32 spin = 0; spin < (1u << 18); spin++) {
    asm volatile("global_load_dwordx4 %0, %1, %2 sc1\n\ts_waitcnt vmcnt(0)" : "=&v"(f) : "v"(voff), "s"(ws) : "memory");
    if (__builtin_amdgcn_ballot_w64(unit_bad(f, tag)) == 0) break;
    __builtin_amdgcn_s_sleep(6);
  }
  return f;
}
DEV void set_line(char* ws, u32 off, u64 tag, int lane, u32 a = 0, u32 b = 0) {
  u32x4 v = {a, (u32)tag, b, (u32)(tag >> 32)};
  __builtin_amdgcn_raw_buffer_store_b128(v, __builtin_amdgcn_make_buffer_rsrc(ws, 0, 0x02000000, 0x00020000), off + lane * 16, 0, 16);
}
DEV bool arrive_last(u32 cnt, u32 n, int lane) {
  u32 old = 0;
  if (lane == 0) old = __hip_atomic_fetch_add((LAS u32*)(size_t)cnt, 1u, __ATOMIC_RELAXED, __HIP_MEMORY_SCOPE_WORKGROUP);
  return (u32)__builtin_amdgcn_readfirstlane(old) == n - 1;
}
struct Params {
  const u16 *x, *wqkv, *wo, *w1, *b1, *w2, *b2, *gw;
  u16* out; char* ws;
};

constexpr u32 P1_SLOT = 32768, P1_LN = 131072;
constexpr u32 LW = 0, AT_RING = 98304, AT_SLOT = 4096, SPARE = AT_RING + 8 * AT_SLOT + 3072, AT_GATE = SPARE + 6 * AT_SLOT;
constexpr u32 C_Q = SPARE + 7 * AT_SLOT, C_KV = C_Q + 4, C_P1 = C_KV + 4, C_G = C_P1 + 4, C_P = C_G + 4, C_O = C_P + 4, C_X = C_O + 4,
              C_S = C_Q + 64, C_U = C_S + 4, C_VAL = C_Q + 128, LDS_BYTES = 163840;

DEV u32 lds_poll(u32 a) { return __builtin_amdgcn_readfirstlane(*(volatile LAS u32*)(size_t)a); }
DEV void garrive(u32 cnt, int lane) { if (lane == 0) __hip_atomic_fetch_add((LAS u32*)(size_t)cnt, 1u, __ATOMIC_RELAXED, __HIP_MEMORY_SCOPE_WORKGROUP); }
DEV void gwait(u32 cnt, u32 target) { while (lds_poll(cnt) < target) __builtin_amdgcn_s_sleep(1); }
DEV int part_group(int g, int part) { return g ^ (part == 0 ? 0 : part == 1 ? 1 : 6); }
DEV u64 make_tag(u32x2 ep) { return (((u64)__builtin_amdgcn_readfirstlane(ep[1]) << 32) | (u32)__builtin_amdgcn_readfirstlane(ep[0])) + 1; }
DEV u64 p1_loader(const Params& p, int tg, int g, int w, int lane, u32 w1off, u32& pw) {
  u32x2 ep;
  const int r = lane >> 3;
  const u32 wv = ((w - 8) * 16 + r) * 3072 + (((lane & 7) ^ (((r >> 1) & 1) << 2)) << 4);
  const u32 xv = (tg * 64 + (w - 8) * 16 + r) * 1024 + (((lane & 7) ^ (r >> 1)) << 4);
  auto issue = [&](int kc) {
    u32 slot = (kc & 3) * P1_SLOT + (w - 8) * 2048;
    const char* b = (const char*)p.wqkv + kc * 64 * 3072;
    const char* xb = (const char*)p.x + kc * 128;
    glds16(xb, xv, slot + 24576); glds16(xb + 8192, xv ^ 64u, slot + 24576 + 1024);
    #pragma unroll
    for (int q = 0; q < 3; q++) { const char* bq = b + q * 1024 + part_group(g, q) * 128; glds16(bq, wv, slot + q * 8192); glds16(bq + 8 * 3072, wv, slot + q * 8192 + 1024); }
  };
  issue(0); issue(1);
  u32 pf;
  {
    const int e = (w - 8) * 36 + (lane < 36 ? lane : 0), c = e % 12;
    warm(pf, p.wqkv, (128 + (tg & 7) * 4 + (g & 3) + 32 * (e / 12)) * 3072 + (c >> 2) * 1024 + ((g & 4) ^ (c >= 8 ? 4 : 0)) * 128 + (c & 3) * 128);
  }
  asm volatile("global_load_dwordx2 %0, %1, %2 sc1" : "=v"(ep) : "v"((u32)WS_EPOCH), "s"(p.ws) : "memory");
  #pragma unroll
  for (int it = 0; it < 8; it++) {
    if (it == 0) VMWAIT(10); else if (it == 1) VMWAIT(2);
    else if (it < 6) VMWAIT(16); else if (it == 6) VMWAIT(9); else VMWAIT(1);
    raw_barrier();
    if (it == 1) { issue(2); issue(3); }
    if (it >= 1 && it + 3 < 8) issue(it + 3);
    if (it == 5) warm(pw, p.w1, w1off);
  }
  raw_barrier();
  asm volatile("" : "+v"(ep), "+v"(pf));
  return make_tag(ep);
}
template <bool ISV>
DEV u64 p1_compute(const Params& p, int tg, int g, int w, int lane, u32& cpf) {
  u32x2 ep; u64 tag = 0;
  asm volatile("global_load_dwordx2 %0, %1, %2 sc1" : "=v"(ep) : "v"((u32)WS_EPOCH), "s"(p.ws) : "memory");
  const int part = ISV ? 2 : (w >> 1), jc = w & 1, tv = (w >> 1) & 1;
  const int c31 = lane & 31, h = lane >> 5;
  u32 xa[4], wa0, wa1;
  for (int k4 = 0; k4 < 4; k4++) xa[k4] = 24576 + ((ISV ? tv * 32 : 0) + c31) * 128 + (((2 * k4 + h) ^ ((c31 >> 1) & 7)) << 4);
  {
    int g4 = (lane >> 4) & 1, i = lane & 15, q = i >> 2;
    int r0 = 8 * h + q, r1 = r0 + 4, ch = 4 * jc + 2 * g4 + ((i & 3) >> 1);
    wa0 = part * 8192 + r0 * 128 + ((ch ^ (((r0 >> 1) & 1) << 2)) << 4) + (i & 1) * 8;
    wa1 = part * 8192 + r1 * 128 + ((ch ^ (((r1 >> 1) & 1) << 2)) << 4) + (i & 1) * 8;
  }
  VMWAIT(1);
  const u32x4 xr = lds_r128(P1_LN + w * 1024 + lane * 16), gr = lds_r128(P1_LN + 8192 + w * 1024 + lane * 16);
  {
    float v[8], s = 0.f, gs = 0.f;
    for (int i = 0; i < 4; i++) { v[2 * i] = bf_lo(xr[i]); v[2 * i + 1] = bf_hi(xr[i]); s += v[2 * i] + v[2 * i + 1]; gs += v[2 * i] * bf_lo(gr[i]) + v[2 * i + 1] * bf_hi(gr[i]); }
    float mean = wave_sum(s) * (1.0f / 512.0f); gs = wave_sum(gs);
    float q = 0.f;
    for (int i = 0; i < 8; i++) { v[i] -= mean; q += v[i] * v[i]; }
    float rstd = __builtin_amdgcn_rsqf(wave_sum(q) * (1.0f / 512.0f) + 1e-5f);
    u32x4 hh = {pack2(v[0] * rstd, v[1] * rstd), pack2(v[2] * rstd, v[3] * rstd), pack2(v[4] * rstd, v[5] * rstd), pack2(v[6] * rstd, v[7] * rstd)};
    *(LAS u32x4*)(size_t)(P1_LN + w * 1024 + lane * 16) = hh;
    const float gate = __builtin_amdgcn_rcpf(1.0f + __builtin_amdgcn_exp2f(-1.4426950408889634f * gs));
    if (lane == 0) *(LAS float*)(size_t)(C_VAL + w * 4) = gate;
  }
  f32x16 acc0 = {0}, acc1 = {0};
  u32x4 xf[2][4], xg[2][4], wf[2][4];
  #pragma unroll
  for (int it = 0; it < 9; it++) {
    if (it == 8) {
      asm volatile("s_waitcnt vmcnt(1)" : "+v"(ep), "+v"(cpf) :: "memory");
      tag = make_tag(ep);
    }
    raw_barrier();
    const u32 slot = (it & 3) * P1_SLOT;
    const int c = it & 1;
    auto rd = [&](int k4) {
      if (it < 8) {
        wf[c][k4] = lds_tr2(slot + wa0 + k4 * 2048, slot + wa1 + k4 * 2048); xf[c][k4] = lds_r128(slot + xa[k4]);
        if (!ISV) xg[c][k4] = lds_r128(slot + xa[k4] + 4096);
      }
      __builtin_amdgcn_sched_barrier(0);
    };
    auto mm = [&](int k4, int t) {
      if (it > 0) {
        if (ISV) acc0 = mfma32(xf[c ^ 1][k4], wf[c ^ 1][k4], acc0);
        else if (t == 0) acc0 = mfma32(wf[c ^ 1][k4], xf[c ^ 1][k4], acc0);
        else acc1 = mfma32(wf[c ^ 1][k4], xg[c ^ 1][k4], acc1);
      }
      __builtin_amdgcn_sched_barrier(0);
    };
    if (ISV) { mm(0, 0); rd(0); rd(1); mm(1, 0); rd(2); rd(3); mm(2, 0); mm(3, 0); }
    else { mm(0, 0); rd(0); mm(0, 1); rd(1); mm(1, 0); rd(2); mm(1, 1); rd(3); mm(2, 0); mm(2, 1); mm(3, 0); mm(3, 1); }
    if (it == 0) {
      int d = w * 64 + lane; u32 t[8];
      for (int j = 0; j < 8; j++) t[j] = *(const LAS u16*)(size_t)(P1_LN + j * 1024 + d * 2);
      u32x4 o = {t[0] | (t[1] << 16), t[2] | (t[3] << 16), t[4] | (t[5] << 16), t[6] | (t[7] << 16)};
      st16_wt(p.ws + WS_H, (tg * 8 + g) * 8192 + d * 16, o);
    }
  }
  if (!ISV) {
    if (part == 0) for (int i = 0; i < 16; i++) { acc0[i] *= QSCALE; acc1[i] *= QSCALE; }
    const int head = 4 * part_group(g, part) + 2 * jc;
    const int slot = (part == 0) ? lane : ((lane & ~12) | ((lane & 4) << 1) | ((lane & 8) >> 1));
    char* dst = p.ws + (part == 0 ? WS_Q : WS_K);
    const u32 o = ((head * 64 + tg * 2) * 64 + slot) * 16;
    st16_wt(dst, o, u32x4{pack2(acc0[0], acc0[1]), pack2(acc0[2], acc0[3]), pack2(acc0[4], acc0[5]), pack2(acc0[6], acc0[7])});
    st16_wt(dst, o + 65536, u32x4{pack2(acc0[8], acc0[9]), pack2(acc0[10], acc0[11]), pack2(acc0[12], acc0[13]), pack2(acc0[14], acc0[15])});
    st16_wt(dst, o + 1024, u32x4{pack2(acc1[0], acc1[1]), pack2(acc1[2], acc1[3]), pack2(acc1[4], acc1[5]), pack2(acc1[6], acc1[7])});
    st16_wt(dst, o + 65536 + 1024, u32x4{pack2(acc1[8], acc1[9]), pack2(acc1[10], acc1[11]), pack2(acc1[12], acc1[13]), pack2(acc1[14], acc1[15])});
    __builtin_amdgcn_sched_barrier(0);
    float a0 = 0.f, a1 = 0.f, b0 = 0.f, b1 = 0.f;
    for (int i = 0; i < 8; i++) { a0 += acc0[i] * acc0[i]; a1 += acc0[8 + i] * acc0[8 + i]; b0 += acc1[i] * acc1[i]; b1 += acc1[8 + i] * acc1[8 + i]; }
    a0 += xhalf(a0, lane); a1 += xhalf(a1, lane); b0 += xhalf(b0, lane); b1 += xhalf(b1, lane);
    float n0 = row16_max(fmaxf(a0, b0)), n1 = row16_max(fmaxf(a1, b1));
    n0 = fmaxf(rdlane(n0, 0), rdlane(n0, 16)); n1 = fmaxf(rdlane(n1, 0), rdlane(n1, 16));
    if (lane < 2) *(LAS float*)(size_t)(C_VAL + 32 + (part * 4 + 2 * jc + lane) * 4) = lane ? n1 : n0;
  } else {
    const int head = 4 * part_group(g, 2) + 2 * jc + (c31 >> 4);
    const u32 o = ((head * 64 + tg * 2 + tv) * 64 + h * 16 + (c31 & 15)) * 16;
    st16_wt(p.ws + WS_V, o, u32x4{pack2(acc0[0], acc0[1]), pack2(acc0[2], acc0[3]), pack2(acc0[4], acc0[5]), pack2(acc0[6], acc0[7])});
    st16_wt(p.ws + WS_V, o + 512, u32x4{pack2(acc0[8], acc0[9]), pack2(acc0[10], acc0[11]), pack2(acc0[12], acc0[13]), pack2(acc0[14], acc0[15])});
  }
  asm volatile("s_waitcnt vmcnt(0) lgkmcnt(0)" ::: "memory");
  if (arrive_last(C_P1, 8, lane) && lane < 8) {
    const u32x2 v = *(const LAS u32x2*)(size_t)(C_VAL + lane * 8);
    set_line(p.ws, WS_F1 + (tg * 8 + g) * 128, tag, lane, v[0], v[1]);
  }
  return tag;
}

DEV void attn_wave(const Params& p, int head, int qb, int w, int lane) {
  u32 ov = ((lane & 27) == 16) ? 0x3f803f80u : 0u;
  u32 gk = lane * 16, gv = ((lane & 15) + ((lane >> 5) << 4)) * 16;
  const char* kb = p.ws + WS_K + (size_t)(head * 64 + w * 8 + 4) * 1024;
  const char* vb = p.ws + WS_V + (size_t)(head * 64 + w * 8 + 4) * 1024;
  const char* qp = p.ws + WS_Q + (size_t)(head * 64 + qb * 8 + 2 * (w & 3)) * 1024;
  asm volatile(
  "s_mov_b32 s76, 0x0000ffff\n\t"
  "s_mov_b32 s77, 0x0000ffff\n\t"
  "s_setprio 2\n\t"
  "s_cmp_lg_u32 %[rb], 0\n\t"
  "s_cbranch_scc1 Lgo_%=\n\t"
  "s_setprio 0\n\t"
  "s_sleep 28\n\t"
  "Lgo_%=:\n\t"
  "global_load_dwordx4 v[144:147], v160, %[qb] sc1\n\t"
  "global_load_dwordx4 v[148:151], v160, %[qb] offset:1024 sc1\n\t"
  "global_load_dwordx4 v[48:51], v160, %[kb] offset:-4096 sc1\n\t"
  "s_mov_b64 exec, s[76:77]\n\t"
  "global_load_dwordx4 v[80:83], v161, %[vb] offset:-4096 sc1\n\t"
  "s_mov_b64 exec, -1\n\t"
  "global_load_dwordx4 v[52:55], v160, %[kb] offset:-3072 sc1\n\t"
  "s_mov_b64 exec, s[76:77]\n\t"
  "global_load_dwordx4 v[84:87], v161, %[vb] offset:-3584 sc1\n\t"
  "global_load_dwordx4 v[88:91], v161, %[vb] offset:-3072 sc1\n\t"
  "s_mov_b64 exec, -1\n\t"
  "global_load_dwordx4 v[56:59], v160, %[kb] offset:-2048 sc1\n\t"
  "s_mov_b64 exec, s[76:77]\n\t"
  "global_load_dwordx4 v[92:95], v161, %[vb] offset:-2560 sc1\n\t"
  "global_load_dwordx4 v[96:99], v161, %[vb] offset:-2048 sc1\n\t"
  "s_mov_b64 exec, -1\n\t"
  "global_load_dwordx4 v[60:63], v160, %[kb] offset:-1024 sc1\n\t"
  "s_mov_b64 exec, s[76:77]\n\t"
  "global_load_dwordx4 v[100:103], v161, %[vb] offset:-1536 sc1\n\t"
  "global_load_dwordx4 v[104:107], v161, %[vb] offset:-1024 sc1\n\t"
  "s_mov_b64 exec, -1\n\t"
  "global_load_dwordx4 v[64:67], v160, %[kb] offset:0 sc1\n\t"
  "s_mov_b64 exec, s[76:77]\n\t"
  "global_load_dwordx4 v[108:111], v161, %[vb] offset:-512 sc1\n\t"
  "global_load_dwordx4 v[112:115], v161, %[vb] offset:0 sc1\n\t"
  "s_mov_b64 exec, -1\n\t"
  "global_load_dwordx4 v[68:71], v160, %[kb] offset:1024 sc1\n\t"
  "s_cbranch_scc1 Lup_%=\n\t"
  "s_mov_b64 exec, s[76:77]\n\t"
  "global_load_dwordx4 v[116:119], v161, %[vb] offset:512 sc1\n\t"
  "global_load_dwordx4 v[120:123], v161, %[vb] offset:1024 sc1\n\t"
  "s_mov_b64 exec, -1\n\t"
  "global_load_dwordx4 v[72:75], v160, %[kb] offset:2048 sc1\n\t"
  "s_mov_b64 exec, s[76:77]\n\t"
  "global_load_dwordx4 v[124:127], v161, %[vb] offset:1536 sc1\n\t"
  "global_load_dwordx4 v[128:131], v161, %[vb] offset:2048 sc1\n\t"
  "s_mov_b64 exec, -1\n\t"
  "global_load_dwordx4 v[76:79], v160, %[kb] offset:3072 sc1\n\t"
  "s_mov_b64 exec, s[76:77]\n\t"
  "global_load_dwordx4 v[132:135], v161, %[vb] offset:2560 sc1\n\t"
  "global_load_dwordx4 v[136:139], v161, %[vb] offset:3072 sc1\n\t"
  "s_mov_b64 exec, -1\n\t"
  "s_mov_b64 exec, s[76:77]\n\t"
  "global_load_dwordx4 v[140:143], v161, %[vb] offset:3584 sc1\n\t"
  "s_mov_b64 exec, -1\n\t"
  "Lup_%=:\n\t"
  "s_not_b64 exec, s[76:77]\n\t"
  "v_mov_b32_e32 v2, v162\n\t"
  "v_mov_b32_e32 v3, v162\n\t"
  "v_mov_b64_e32 v[80:81], v[2:3]\n\t"
  "v_mov_b64_e32 v[82:83], v[2:3]\n\t"
  "v_mov_b64_e32 v[84:85], v[2:3]\n\t"
  "v_mov_b64_e32 v[86:87], v[2:3]\n\t"
  "v_mov_b64_e32 v[88:89], v[2:3]\n\t"
  "v_mov_b64_e32 v[90:91], v[2:3]\n\t"
  "v_mov_b64_e32 v[92:93], v[2:3]\n\t"
  "v_mov_b64_e32 v[94:95], v[2:3]\n\t"
  "v_mov_b64_e32 v[96:97], v[2:3]\n\t"
  "v_mov_b64_e32 v[98:99], v[2:3]\n\t"
  "v_mov_b64_e32 v[100:101], v[2:3]\n\t"
  "v_mov_b64_e32 v[102:103], v[2:3]\n\t"
  "v_mov_b64_e32 v[104:105], v[2:3]\n\t"
  "v_mov_b64_e32 v[106:107], v[2:3]\n\t"
  "v_mov_b64_e32 v[108:109], v[2:3]\n\t"
  "v_mov_b64_e32 v[110:111], v[2:3]\n\t"
  "v_mov_b64_e32 v[112:113], v[2:3]\n\t"
  "v_mov_b64_e32 v[114:115], v[2:3]\n\t"
  "v_mov_b64_e32 v[116:117], v[2:3]\n\t"
  "v_mov_b64_e32 v[118:119], v[2:3]\n\t"
  "v_mov_b64_e32 v[120:121], v[2:3]\n\t"
  "v_mov_b64_e32 v[122:123], v[2:3]\n\t"
  "v_mov_b64_e32 v[124:125], v[2:3]\n\t"
  "v_mov_b64_e32 v[126:127], v[2:3]\n\t"
  "v_mov_b64_e32 v[128:129], v[2:3]\n\t"
  "v_mov_b64_e32 v[130:131], v[2:3]\n\t"
  "v_mov_b64_e32 v[132:133], v[2:3]\n\t"
  "v_mov_b64_e32 v[134:135], v[2:3]\n\t"
  "v_mov_b64_e32 v[136:137], v[2:3]\n\t"
  "v_mov_b64_e32 v[138:139], v[2:3]\n\t"
  "v_mov_b64_e32 v[140:141], v[2:3]\n\t"
  "v_mov_b64_e32 v[142:143], v[2:3]\n\t"
  "s_mov_b64 exec, -1\n\t"
  "v_mov_b64_e32 v[0:1], 0\n\t"
  "v_mov_b64_e32 v[2:3], 0\n\t"
  "v_mov_b64_e32 v[4:5], 0\n\t"
  "v_mov_b64_e32 v[6:7], 0\n\t"
  "v_mov_b64_e32 v[8:9], 0\n\t"
  "v_mov_b32_e32 v152, %[qc]\n\t"
  "v_mov_b32_e32 v154, 1\n\t"
  "s_cmp_lg_u32 %[rb], 0\n\t"
  "s_cbranch_scc0 Lnz_%=\n\t"
  "v_add_u32_e32 v153, 0xffff8020, v165\n\t"
  "ds_write_b128 v153, v[0:3] offset:0\n\t"
  "ds_write_b128 v153, v[0:3] offset:4096\n\t"
  "ds_write_b128 v153, v[0:3] offset:32768\n\t"
  "ds_write_b128 v153, v[0:3] offset:36864\n\t"
  "Lnz_%=:\n\t"
  "s_cbranch_scc0 Lnq_%=\n\t"
  "s_waitcnt vmcnt(14)\n\t"
  "v_mfma_f32_32x32x16_bf16 v[16:31], v[48:51], v[144:147], 0\n\t"
  "ds_write_b128 v166, v[144:147]\n\t"
  "ds_write_b128 v166, v[148:151] offset:4096\n\t"
  "s_mov_b64 exec, 1\n\t"
  "ds_add_u32 v152, v154\n\t"
  "s_mov_b64 exec, -1\n\t"
  "s_branch Lnk_%=\n\t"
  "Lnq_%=:\n\t"
  "s_waitcnt vmcnt(0)\n\t"
  "v_mfma_f32_32x32x16_bf16 v[16:31], v[48:51], v[144:147], 0\n\t"
  "s_mov_b64 exec, 1\n\t"
  "ds_add_u32 v152, v154 offset:4\n\t"
  "s_mov_b64 exec, -1\n\t"
  "Lnk_%=:\n\t"
  "s_nop 11\n\t"
  "s_cbranch_scc0 Lsk0_%=\n\t"
  "s_mov_b64 exec, s[76:77]\n\t"
  "global_load_dwordx4 v[116:119], v161, %[vb] offset:512 sc1\n\t"
  "global_load_dwordx4 v[120:123], v161, %[vb] offset:1024 sc1\n\t"
  "s_mov_b64 exec, -1\n\t"
  "global_load_dwordx4 v[72:75], v160, %[kb] offset:2048 sc1\n\t"
  "s_waitcnt vmcnt(15)\n\t"
  "Lsk0_%=:\n\t"
  "v_exp_f32_e32 v16, v16\n\t"
  "v_exp_f32_e32 v17, v17\n\t"
  "v_exp_f32_e32 v18, v18\n\t"
  "v_exp_f32_e32 v19, v19\n\t"
  "v_exp_f32_e32 v20, v20\n\t"
  "v_mfma_f32_32x32x16_bf16 v[32:47], v[52:55], v[144:147], 0\n\t"
  "v_exp_f32_e32 v21, v21\n\t"
  "v_exp_f32_e32 v22, v22\n\t"
  "v_exp_f32_e32 v23, v23\n\t"
  "v_exp_f32_e32 v24, v24\n\t"
  "v_exp_f32_e32 v25, v25\n\t"
  "v_smfmac_f32_32x32x32_bf16 v[0:15], v[80:83], v[16:23], v164\n\t"
  "v_exp_f32_e32 v26, v26\n\t"
  "v_exp_f32_e32 v27, v27\n\t"
  "v_exp_f32_e32 v28, v28\n\t"
  "v_exp_f32_e32 v29, v29\n\t"
  "v_exp_f32_e32 v30, v30\n\t"
  "v_exp_f32_e32 v31, v31\n\t"
  "s_cbranch_scc0 Lsk1_%=\n\t"
  "s_mov_b64 exec, s[76:77]\n\t"
  "global_load_dwordx4 v[124:127], v161, %[vb] offset:1536 sc1\n\t"
  "global_load_dwordx4 v[128:131], v161, %[vb] offset:2048 sc1\n\t"
  "s_mov_b64 exec, -1\n\t"
  "global_load_dwordx4 v[76:79], v160, %[kb] offset:3072 sc1\n\t"
  "s_waitcnt vmcnt(15)\n\t"
  "Lsk1_%=:\n\t"
  "v_exp_f32_e32 v32, v32\n\t"
  "v_exp_f32_e32 v33, v33\n\t"
  "v_smfmac_f32_32x32x32_bf16 v[0:15], v[84:87], v[24:31], v164\n\t"
  "v_exp_f32_e32 v34, v34\n\t"
  "v_exp_f32_e32 v35, v35\n\t"
  "v_exp_f32_e32 v36, v36\n\t"
  "v_mfma_f32_32x32x16_bf16 v[16:31], v[56:59], v[144:147], 0\n\t"
  "v_exp_f32_e32 v37, v37\n\t"
  "v_exp_f32_e32 v38, v38\n\t"
  "v_exp_f32_e32 v39, v39\n\t"
  "v_exp_f32_e32 v40, v40\n\t"
  "v_exp_f32_e32 v41, v41\n\t"
  "v_smfmac_f32_32x32x32_bf16 v[0:15], v[88:91], v[32:39], v164\n\t"
  "v_exp_f32_e32 v42, v42\n\t"
  "v_exp_f32_e32 v43, v43\n\t"
  "v_exp_f32_e32 v44, v44\n\t"
  "v_exp_f32_e32 v45, v45\n\t"
  "v_exp_f32_e32 v46, v46\n\t"
  "v_exp_f32_e32 v47, v47\n\t"
  "s_cbranch_scc0 Lsk2_%=\n\t"
  "s_mov_b64 exec, s[76:77]\n\t"
  "global_load_dwordx4 v[132:135], v161, %[vb] offset:2560 sc1\n\t"
  "global_load_dwordx4 v[136:139], v161, %[vb] offset:3072 sc1\n\t"
  "s_mov_b64 exec, -1\n\t"
  "s_waitcnt vmcnt(14)\n\t"
  "Lsk2_%=:\n\t"
  "v_exp_f32_e32 v16, v16\n\t"
  "v_exp_f32_e32 v17, v17\n\t"
  "v_smfmac_f32_32x32x32_bf16 v[0:15], v[92:95], v[40:47], v164\n\t"
  "v_exp_f32_e32 v18, v18\n\t"
  "v_exp_f32_e32 v19, v19\n\t"
  "v_exp_f32_e32 v20, v20\n\t"
  "v_mfma_f32_32x32x16_bf16 v[32:47], v[60:63], v[144:147], 0\n\t"
  "v_exp_f32_e32 v21, v21\n\t"
  "v_exp_f32_e32 v22, v22\n\t"
  "v_exp_f32_e32 v23, v23\n\t"
  "v_exp_f32_e32 v24, v24\n\t"
  "v_exp_f32_e32 v25, v25\n\t"
  "v_smfmac_f32_32x32x32_bf16 v[0:15], v[96:99], v[16:23], v164\n\t"
  "v_exp_f32_e32 v26, v26\n\t"
  "v_exp_f32_e32 v27, v27\n\t"
  "v_exp_f32_e32 v28, v28\n\t"
  "v_exp_f32_e32 v29, v29\n\t"
  "v_exp_f32_e32 v30, v30\n\t"
  "v_exp_f32_e32 v31, v31\n\t"
  "s_cbranch_scc0 Lsk3_%=\n\t"
  "s_mov_b64 exec, s[76:77]\n\t"
  "global_load_dwordx4 v[140:143], v161, %[vb] offset:3584 sc1\n\t"
  "s_mov_b64 exec, -1\n\t"
  "s_waitcnt vmcnt(12)\n\t"
  "Lsk3_%=:\n\t"
  "v_exp_f32_e32 v32, v32\n\t"
  "v_exp_f32_e32 v33, v33\n\t"
  "v_smfmac_f32_32x32x32_bf16 v[0:15], v[100:103], v[24:31], v164\n\t"
  "v_exp_f32_e32 v34, v34\n\t"
  "v_exp_f32_e32 v35, v35\n\t"
  "v_exp_f32_e32 v36, v36\n\t"
  "v_mfma_f32_32x32x16_bf16 v[16:31], v[64:67], v[144:147], 0\n\t"
  "v_exp_f32_e32 v37, v37\n\t"
  "v_exp_f32_e32 v38, v38\n\t"
  "v_exp_f32_e32 v39, v39\n\t"
  "v_exp_f32_e32 v40, v40\n\t"
  "v_exp_f32_e32 v41, v41\n\t"
  "v_smfmac_f32_32x32x32_bf16 v[0:15], v[104:107], v[32:39], v164\n\t"
  "v_exp_f32_e32 v42, v42\n\t"
  "v_exp_f32_e32 v43, v43\n\t"
  "v_exp_f32_e32 v44, v44\n\t"
  "v_exp_f32_e32 v45, v45\n\t"
  "v_exp_f32_e32 v46, v46\n\t"
  "v_exp_f32_e32 v47, v47\n\t"
  "s_cbranch_scc0 Lsk4_%=\n\t"
  "s_waitcnt vmcnt(9)\n\t"
  "Lsk4_%=:\n\t"
  "v_exp_f32_e32 v16, v16\n\t"
  "v_exp_f32_e32 v17, v17\n\t"
  "v_smfmac_f32_32x32x32_bf16 v[0:15], v[108:111], v[40:47], v164\n\t"
  "v_exp_f32_e32 v18, v18\n\t"
  "v_exp_f32_e32 v19, v19\n\t"
  "v_exp_f32_e32 v20, v20\n\t"
  "v_mfma_f32_32x32x16_bf16 v[32:47], v[68:71], v[144:147], 0\n\t"
  "v_exp_f32_e32 v21, v21\n\t"
  "v_exp_f32_e32 v22, v22\n\t"
  "v_exp_f32_e32 v23, v23\n\t"
  "v_exp_f32_e32 v24, v24\n\t"
  "v_exp_f32_e32 v25, v25\n\t"
  "v_smfmac_f32_32x32x32_bf16 v[0:15], v[112:115], v[16:23], v164\n\t"
  "v_exp_f32_e32 v26, v26\n\t"
  "v_exp_f32_e32 v27, v27\n\t"
  "v_exp_f32_e32 v28, v28\n\t"
  "v_exp_f32_e32 v29, v29\n\t"
  "v_exp_f32_e32 v30, v30\n\t"
  "v_exp_f32_e32 v31, v31\n\t"
  "s_cbranch_scc0 Lsk5_%=\n\t"
  "s_waitcnt vmcnt(6)\n\t"
  "Lsk5_%=:\n\t"
  "v_exp_f32_e32 v32, v32\n\t"
  "v_exp_f32_e32 v33, v33\n\t"
  "v_smfmac_f32_32x32x32_bf16 v[0:15], v[116:119], v[24:31], v164\n\t"
  "v_exp_f32_e32 v34, v34\n\t"
  "v_exp_f32_e32 v35, v35\n\t"
  "v_exp_f32_e32 v36, v36\n\t"
  "v_mfma_f32_32x32x16_bf16 v[16:31], v[72:75], v[144:147], 0\n\t"
  "v_exp_f32_e32 v37, v37\n\t"
  "v_exp_f32_e32 v38, v38\n\t"
  "v_xor_b32_e32 v153, 0x1000, v166\n\t"
  "ds_read_b32 v154, v152\n\t"
  "v_exp_f32_e32 v39, v39\n\t"
  "v_exp_f32_e32 v40, v40\n\t"
  "v_exp_f32_e32 v41, v41\n\t"
  "v_smfmac_f32_32x32x32_bf16 v[0:15], v[120:123], v[32:39], v164\n\t"
  "v_exp_f32_e32 v42, v42\n\t"
  "v_exp_f32_e32 v43, v43\n\t"
  "v_exp_f32_e32 v44, v44\n\t"
  "v_exp_f32_e32 v45, v45\n\t"
  "v_exp_f32_e32 v46, v46\n\t"
  "s_waitcnt lgkmcnt(0)\n\t"
  "v_cmp_gt_u32_e32 vcc, 4, v154\n\t"
  "s_cbranch_vccnz Lqs_%=\n\t"
  "Lqr_%=:\n\t"
  "v_exp_f32_e32 v47, v47\n\t"
  "s_cbranch_scc0 Lsk6_%=\n\t"
  "s_waitcnt vmcnt(3)\n\t"
  "Lsk6_%=:\n\t"
  "v_exp_f32_e32 v16, v16\n\t"
  "v_exp_f32_e32 v17, v17\n\t"
  "v_smfmac_f32_32x32x32_bf16 v[0:15], v[124:127], v[40:47], v164\n\t"
  "v_exp_f32_e32 v18, v18\n\t"
  "v_exp_f32_e32 v19, v19\n\t"
  "v_exp_f32_e32 v20, v20\n\t"
  "v_mfma_f32_32x32x16_bf16 v[32:47], v[76:79], v[144:147], 0\n\t"
  "ds_read_b128 v[144:147], v153\n\t"
  "v_exp_f32_e32 v21, v21\n\t"
  "v_exp_f32_e32 v22, v22\n\t"
  "v_exp_f32_e32 v23, v23\n\t"
  "v_exp_f32_e32 v24, v24\n\t"
  "v_exp_f32_e32 v25, v25\n\t"
  "v_smfmac_f32_32x32x32_bf16 v[0:15], v[128:131], v[16:23], v164\n\t"
  "v_exp_f32_e32 v26, v26\n\t"
  "v_exp_f32_e32 v27, v27\n\t"
  "v_exp_f32_e32 v28, v28\n\t"
  "v_exp_f32_e32 v29, v29\n\t"
  "v_exp_f32_e32 v30, v30\n\t"
  "v_exp_f32_e32 v31, v31\n\t"
  "s_cbranch_scc0 Lsk7_%=\n\t"
  "s_waitcnt vmcnt(1)\n\t"
  "Lsk7_%=:\n\t"
  "v_exp_f32_e32 v32, v32\n\t"
  "v_exp_f32_e32 v33, v33\n\t"
  "v_smfmac_f32_32x32x32_bf16 v[0:15], v[132:135], v[24:31], v164\n\t"
  "v_exp_f32_e32 v34, v34\n\t"
  "v_exp_f32_e32 v35, v35\n\t"
  "v_exp_f32_e32 v36, v36\n\t"
  "s_waitcnt lgkmcnt(0)\n\t"
  "v_mfma_f32_32x32x16_bf16 v[16:31], v[48:51], v[144:147], 0\n\t"
  "v_exp_f32_e32 v37, v37\n\t"
  "v_exp_f32_e32 v38, v38\n\t"
  "v_exp_f32_e32 v39, v39\n\t"
  "v_exp_f32_e32 v40, v40\n\t"
  "v_exp_f32_e32 v41, v41\n\t"
  "v_smfmac_f32_32x32x32_bf16 v[0:15], v[136:139], v[32:39], v164\n\t"
  "v_exp_f32_e32 v42, v42\n\t"
  "v_exp_f32_e32 v43, v43\n\t"
  "s_cbranch_scc0 Lkv_%=\n\t"
  "s_waitcnt vmcnt(0)\n\t"
  "v_mov_b32_e32 v154, 1\n\t"
  "s_mov_b64 exec, 1\n\t"
  "ds_add_u32 v152, v154 offset:4\n\t"
  "s_mov_b64 exec, -1\n\t"
  "Lkv_%=:\n\t"
  "v_exp_f32_e32 v44, v44\n\t"
  "v_exp_f32_e32 v45, v45\n\t"
  "v_mov_b64_e32 v[148:149], 0\n\t"
  "v_mov_b64_e32 v[150:151], 0\n\t"
  "v_mov_b64_e32 v[152:153], 0\n\t"
  "v_mov_b64_e32 v[154:155], 0\n\t"
  "v_mov_b64_e32 v[156:157], 0\n\t"
  "v_exp_f32_e32 v46, v46\n\t"
  "v_exp_f32_e32 v47, v47\n\t"
  "v_exp_f32_e32 v16, v16\n\t"
  "v_exp_f32_e32 v17, v17\n\t"
  "v_smfmac_f32_32x32x32_bf16 v[0:15], v[140:143], v[40:47], v164\n\t"
  "v_exp_f32_e32 v18, v18\n\t"
  "v_exp_f32_e32 v19, v19\n\t"
  "v_exp_f32_e32 v20, v20\n\t"
  "v_mfma_f32_32x32x16_bf16 v[32:47], v[52:55], v[144:147], 0\n\t"
  "v_exp_f32_e32 v21, v21\n\t"
  "v_exp_f32_e32 v22, v22\n\t"
  "v_exp_f32_e32 v23, v23\n\t"
  "v_exp_f32_e32 v24, v24\n\t"
  "v_exp_f32_e32 v25, v25\n\t"
  "v_smfmac_f32_32x32x32_bf16 v[148:163], v[80:83], v[16:23], v164\n\t"
  "v_exp_f32_e32 v26, v26\n\t"
  "v_exp_f32_e32 v27, v27\n\t"
  "v_exp_f32_e32 v28, v28\n\t"
  "v_mov_b32_e32 v14, v165\n\t"
  "ds_write_b128 v14, v[0:3]\n\t"
  "ds_write_b128 v14, v[4:7] offset:16\n\t"
  "ds_write_b32 v14, v8 offset:32\n\t"
  "ds_write_b32 v14, v164 offset:36\n\t"
  "v_exp_f32_e32 v29, v29\n\t"
  "v_exp_f32_e32 v30, v30\n\t"
  "v_exp_f32_e32 v31, v31\n\t"
  "v_exp_f32_e32 v32, v32\n\t"
  "v_exp_f32_e32 v33, v33\n\t"
  "v_smfmac_f32_32x32x32_bf16 v[148:163], v[84:87], v[24:31], v164\n\t"
  "v_exp_f32_e32 v34, v34\n\t"
  "v_exp_f32_e32 v35, v35\n\t"
  "v_exp_f32_e32 v36, v36\n\t"
  "v_mfma_f32_32x32x16_bf16 v[16:31], v[56:59], v[144:147], 0\n\t"
  "v_exp_f32_e32 v37, v37\n\t"
  "v_exp_f32_e32 v38, v38\n\t"
  "v_exp_f32_e32 v39, v39\n\t"
  "v_exp_f32_e32 v40, v40\n\t"
  "v_exp_f32_e32 v41, v41\n\t"
  "v_smfmac_f32_32x32x32_bf16 v[148:163], v[88:91], v[32:39], v164\n\t"
  "v_exp_f32_e32 v42, v42\n\t"
  "v_exp_f32_e32 v43, v43\n\t"
  "v_exp_f32_e32 v44, v44\n\t"
  "v_exp_f32_e32 v45, v45\n\t"
  "v_exp_f32_e32 v46, v46\n\t"
  "v_exp_f32_e32 v47, v47\n\t"
  "v_exp_f32_e32 v16, v16\n\t"
  "v_exp_f32_e32 v17, v17\n\t"
  "v_smfmac_f32_32x32x32_bf16 v[148:163], v[92:95], v[40:47], v164\n\t"
  "v_exp_f32_e32 v18, v18\n\t"
  "v_exp_f32_e32 v19, v19\n\t"
  "v_exp_f32_e32 v20, v20\n\t"
  "v_mfma_f32_32x32x16_bf16 v[32:47], v[60:63], v[144:147], 0\n\t"
  "v_exp_f32_e32 v21, v21\n\t"
  "v_exp_f32_e32 v22, v22\n\t"
  "v_exp_f32_e32 v23, v23\n\t"
  "v_exp_f32_e32 v24, v24\n\t"
  "v_exp_f32_e32 v25, v25\n\t"
  "v_smfmac_f32_32x32x32_bf16 v[148:163], v[96:99], v[16:23], v164\n\t"
  "v_exp_f32_e32 v26, v26\n\t"
  "v_exp_f32_e32 v27, v27\n\t"
  "v_exp_f32_e32 v28, v28\n\t"
  "v_exp_f32_e32 v29, v29\n\t"
  "v_exp_f32_e32 v30, v30\n\t"
  "v_exp_f32_e32 v31, v31\n\t"
  "v_exp_f32_e32 v32, v32\n\t"
  "v_exp_f32_e32 v33, v33\n\t"
  "v_smfmac_f32_32x32x32_bf16 v[148:163], v[100:103], v[24:31], v164\n\t"
  "v_exp_f32_e32 v34, v34\n\t"
  "v_exp_f32_e32 v35, v35\n\t"
  "v_exp_f32_e32 v36, v36\n\t"
  "v_mfma_f32_32x32x16_bf16 v[16:31], v[64:67], v[144:147], 0\n\t"
  "v_exp_f32_e32 v37, v37\n\t"
  "v_exp_f32_e32 v38, v38\n\t"
  "v_xor_b32_e32 v13, 0x2000, v166\n\t"
  "v_exp_f32_e32 v39, v39\n\t"
  "v_exp_f32_e32 v40, v40\n\t"
  "v_exp_f32_e32 v41, v41\n\t"
  "v_smfmac_f32_32x32x32_bf16 v[148:163], v[104:107], v[32:39], v164\n\t"
  "v_exp_f32_e32 v42, v42\n\t"
  "v_exp_f32_e32 v43, v43\n\t"
  "v_exp_f32_e32 v44, v44\n\t"
  "v_exp_f32_e32 v45, v45\n\t"
  "v_exp_f32_e32 v46, v46\n\t"
  "v_exp_f32_e32 v47, v47\n\t"
  "v_exp_f32_e32 v16, v16\n\t"
  "v_exp_f32_e32 v17, v17\n\t"
  "v_smfmac_f32_32x32x32_bf16 v[148:163], v[108:111], v[40:47], v164\n\t"
  "v_exp_f32_e32 v18, v18\n\t"
  "v_exp_f32_e32 v19, v19\n\t"
  "v_exp_f32_e32 v20, v20\n\t"
  "v_mfma_f32_32x32x16_bf16 v[32:47], v[68:71], v[144:147], 0\n\t"
  "v_exp_f32_e32 v21, v21\n\t"
  "v_exp_f32_e32 v22, v22\n\t"
  "v_xor_b32_e32 v14, 0x2000, v165\n\t"
  "ds_read_b32 v15, v14 offset:36\n\t"
  "v_exp_f32_e32 v23, v23\n\t"
  "v_exp_f32_e32 v24, v24\n\t"
  "v_exp_f32_e32 v25, v25\n\t"
  "v_smfmac_f32_32x32x32_bf16 v[148:163], v[112:115], v[16:23], v164\n\t"
  "v_exp_f32_e32 v26, v26\n\t"
  "v_exp_f32_e32 v27, v27\n\t"
  "v_exp_f32_e32 v28, v28\n\t"
  "v_exp_f32_e32 v29, v29\n\t"
  "v_exp_f32_e32 v30, v30\n\t"
  "v_exp_f32_e32 v31, v31\n\t"
  "v_exp_f32_e32 v32, v32\n\t"
  "v_exp_f32_e32 v33, v33\n\t"
  "v_smfmac_f32_32x32x32_bf16 v[148:163], v[116:119], v[24:31], v164\n\t"
  "v_exp_f32_e32 v34, v34\n\t"
  "v_exp_f32_e32 v35, v35\n\t"
  "v_exp_f32_e32 v36, v36\n\t"
  "v_mfma_f32_32x32x16_bf16 v[16:31], v[72:75], v[144:147], 0\n\t"
  "v_exp_f32_e32 v37, v37\n\t"
  "v_exp_f32_e32 v38, v38\n\t"
  "s_waitcnt lgkmcnt(0)\n\t"
  "v_cmp_eq_u32_e32 vcc, 0, v15\n\t"
  "s_cbranch_vccnz Ls1_%=\n\t"
  "Lr1_%=:\n\t"
  "ds_read_b128 v[0:3], v14\n\t"
  "ds_read_b128 v[4:7], v14 offset:16\n\t"
  "ds_read_b32 v8, v14 offset:32\n\t"
  "v_exp_f32_e32 v39, v39\n\t"
  "v_exp_f32_e32 v40, v40\n\t"
  "v_exp_f32_e32 v41, v41\n\t"
  "v_smfmac_f32_32x32x32_bf16 v[148:163], v[120:123], v[32:39], v164\n\t"
  "v_exp_f32_e32 v42, v42\n\t"
  "v_exp_f32_e32 v43, v43\n\t"
  "v_exp_f32_e32 v44, v44\n\t"
  "v_exp_f32_e32 v45, v45\n\t"
  "v_exp_f32_e32 v46, v46\n\t"
  "v_exp_f32_e32 v47, v47\n\t"
  "v_exp_f32_e32 v16, v16\n\t"
  "v_exp_f32_e32 v17, v17\n\t"
  "v_smfmac_f32_32x32x32_bf16 v[148:163], v[124:127], v[40:47], v164\n\t"
  "v_exp_f32_e32 v18, v18\n\t"
  "v_exp_f32_e32 v19, v19\n\t"
  "v_exp_f32_e32 v20, v20\n\t"
  "v_mfma_f32_32x32x16_bf16 v[32:47], v[76:79], v[144:147], 0\n\t"
  "ds_read_b128 v[144:147], v13\n\t"
  "v_exp_f32_e32 v21, v21\n\t"
  "v_exp_f32_e32 v22, v22\n\t"
  "v_exp_f32_e32 v23, v23\n\t"
  "v_exp_f32_e32 v24, v24\n\t"
  "v_exp_f32_e32 v25, v25\n\t"
  "v_smfmac_f32_32x32x32_bf16 v[148:163], v[128:131], v[16:23], v164\n\t"
  "v_exp_f32_e32 v26, v26\n\t"
  "v_exp_f32_e32 v27, v27\n\t"
  "v_exp_f32_e32 v28, v28\n\t"
  "v_exp_f32_e32 v29, v29\n\t"
  "v_exp_f32_e32 v30, v30\n\t"
  "v_exp_f32_e32 v31, v31\n\t"
  "v_exp_f32_e32 v32, v32\n\t"
  "v_exp_f32_e32 v33, v33\n\t"
  "v_smfmac_f32_32x32x32_bf16 v[148:163], v[132:135], v[24:31], v164\n\t"
  "v_exp_f32_e32 v34, v34\n\t"
  "v_exp_f32_e32 v35, v35\n\t"
  "v_exp_f32_e32 v36, v36\n\t"
  "s_waitcnt lgkmcnt(0)\n\t"
  "v_mfma_f32_32x32x16_bf16 v[16:31], v[48:51], v[144:147], 0\n\t"
  "v_exp_f32_e32 v37, v37\n\t"
  "v_exp_f32_e32 v38, v38\n\t"
  "v_exp_f32_e32 v39, v39\n\t"
  "v_exp_f32_e32 v40, v40\n\t"
  "v_exp_f32_e32 v41, v41\n\t"
  "v_smfmac_f32_32x32x32_bf16 v[148:163], v[136:139], v[32:39], v164\n\t"
  "v_exp_f32_e32 v42, v42\n\t"
  "v_exp_f32_e32 v43, v43\n\t"
  "v_exp_f32_e32 v44, v44\n\t"
  "v_exp_f32_e32 v45, v45\n\t"
  "v_exp_f32_e32 v46, v46\n\t"
  "v_exp_f32_e32 v47, v47\n\t"
  "s_setprio 2\n\t"
  "v_exp_f32_e32 v16, v16\n\t"
  "v_exp_f32_e32 v17, v17\n\t"
  "v_smfmac_f32_32x32x32_bf16 v[148:163], v[140:143], v[40:47], v164\n\t"
  "v_exp_f32_e32 v18, v18\n\t"
  "v_exp_f32_e32 v19, v19\n\t"
  "v_exp_f32_e32 v20, v20\n\t"
  "v_mfma_f32_32x32x16_bf16 v[32:47], v[52:55], v[144:147], 0\n\t"
  "v_exp_f32_e32 v21, v21\n\t"
  "v_exp_f32_e32 v22, v22\n\t"
  "v_exp_f32_e32 v23, v23\n\t"
  "v_exp_f32_e32 v24, v24\n\t"
  "v_exp_f32_e32 v25, v25\n\t"
  "v_smfmac_f32_32x32x32_bf16 v[0:15], v[80:83], v[16:23], v164\n\t"
  "v_exp_f32_e32 v26, v26\n\t"
  "v_exp_f32_e32 v27, v27\n\t"
  "v_exp_f32_e32 v28, v28\n\t"
  "v_xor_b32_e32 v162, 0x1000, v165\n\t"
  "ds_write_b128 v162, v[148:151]\n\t"
  "ds_write_b128 v162, v[152:155] offset:16\n\t"
  "ds_write_b32 v162, v156 offset:32\n\t"
  "ds_write_b32 v162, v164 offset:36\n\t"
  "v_exp_f32_e32 v29, v29\n\t"
  "v_exp_f32_e32 v30, v30\n\t"
  "v_exp_f32_e32 v31, v31\n\t"
  "v_exp_f32_e32 v32, v32\n\t"
  "v_exp_f32_e32 v33, v33\n\t"
  "v_smfmac_f32_32x32x32_bf16 v[0:15], v[84:87], v[24:31], v164\n\t"
  "v_exp_f32_e32 v34, v34\n\t"
  "v_exp_f32_e32 v35, v35\n\t"
  "v_exp_f32_e32 v36, v36\n\t"
  "v_mfma_f32_32x32x16_bf16 v[16:31], v[56:59], v[144:147], 0\n\t"
  "v_exp_f32_e32 v37, v37\n\t"
  "v_exp_f32_e32 v38, v38\n\t"
  "v_exp_f32_e32 v39, v39\n\t"
  "v_exp_f32_e32 v40, v40\n\t"
  "v_exp_f32_e32 v41, v41\n\t"
  "v_smfmac_f32_32x32x32_bf16 v[0:15], v[88:91], v[32:39], v164\n\t"
  "v_exp_f32_e32 v42, v42\n\t"
  "v_exp_f32_e32 v43, v43\n\t"
  "v_exp_f32_e32 v44, v44\n\t"
  "v_exp_f32_e32 v45, v45\n\t"
  "v_exp_f32_e32 v46, v46\n\t"
  "v_exp_f32_e32 v47, v47\n\t"
  "v_exp_f32_e32 v16, v16\n\t"
  "v_exp_f32_e32 v17, v17\n\t"
  "v_smfmac_f32_32x32x32_bf16 v[0:15], v[92:95], v[40:47], v164\n\t"
  "v_exp_f32_e32 v18, v18\n\t"
  "v_exp_f32_e32 v19, v19\n\t"
  "v_exp_f32_e32 v20, v20\n\t"
  "v_mfma_f32_32x32x16_bf16 v[32:47], v[60:63], v[144:147], 0\n\t"
  "v_exp_f32_e32 v21, v21\n\t"
  "v_exp_f32_e32 v22, v22\n\t"
  "v_exp_f32_e32 v23, v23\n\t"
  "v_exp_f32_e32 v24, v24\n\t"
  "v_exp_f32_e32 v25, v25\n\t"
  "v_smfmac_f32_32x32x32_bf16 v[0:15], v[96:99], v[16:23], v164\n\t"
  "v_exp_f32_e32 v26, v26\n\t"
  "v_exp_f32_e32 v27, v27\n\t"
  "v_exp_f32_e32 v28, v28\n\t"
  "v_exp_f32_e32 v29, v29\n\t"
  "v_exp_f32_e32 v30, v30\n\t"
  "v_exp_f32_e32 v31, v31\n\t"
  "v_exp_f32_e32 v32, v32\n\t"
  "v_exp_f32_e32 v33, v33\n\t"
  "v_smfmac_f32_32x32x32_bf16 v[0:15], v[100:103], v[24:31], v164\n\t"
  "v_exp_f32_e32 v34, v34\n\t"
  "v_exp_f32_e32 v35, v35\n\t"
  "v_exp_f32_e32 v36, v36\n\t"
  "v_mfma_f32_32x32x16_bf16 v[16:31], v[64:67], v[144:147], 0\n\t"
  "v_exp_f32_e32 v37, v37\n\t"
  "v_exp_f32_e32 v38, v38\n\t"
  "v_xor_b32_e32 v161, 0x3000, v166\n\t"
  "v_exp_f32_e32 v39, v39\n\t"
  "v_exp_f32_e32 v40, v40\n\t"
  "v_exp_f32_e32 v41, v41\n\t"
  "v_smfmac_f32_32x32x32_bf16 v[0:15], v[104:107], v[32:39], v164\n\t"
  "v_exp_f32_e32 v42, v42\n\t"
  "v_exp_f32_e32 v43, v43\n\t"
  "v_exp_f32_e32 v44, v44\n\t"
  "v_exp_f32_e32 v45, v45\n\t"
  "v_exp_f32_e32 v46, v46\n\t"
  "v_exp_f32_e32 v47, v47\n\t"
  "v_exp_f32_e32 v16, v16\n\t"
  "v_exp_f32_e32 v17, v17\n\t"
  "v_smfmac_f32_32x32x32_bf16 v[0:15], v[108:111], v[40:47], v164\n\t"
  "v_exp_f32_e32 v18, v18\n\t"
  "v_exp_f32_e32 v19, v19\n\t"
  "v_exp_f32_e32 v20, v20\n\t"
  "v_mfma_f32_32x32x16_bf16 v[32:47], v[68:71], v[144:147], 0\n\t"
  "v_exp_f32_e32 v21, v21\n\t"
  "v_exp_f32_e32 v22, v22\n\t"
  "v_xor_b32_e32 v162, 0x3000, v165\n\t"
  "ds_read_b32 v163, v162 offset:36\n\t"
  "v_exp_f32_e32 v23, v23\n\t"
  "v_exp_f32_e32 v24, v24\n\t"
  "v_exp_f32_e32 v25, v25\n\t"
  "v_smfmac_f32_32x32x32_bf16 v[0:15], v[112:115], v[16:23], v164\n\t"
  "v_exp_f32_e32 v26, v26\n\t"
  "v_exp_f32_e32 v27, v27\n\t"
  "v_exp_f32_e32 v28, v28\n\t"
  "v_exp_f32_e32 v29, v29\n\t"
  "v_exp_f32_e32 v30, v30\n\t"
  "v_exp_f32_e32 v31, v31\n\t"
  "v_exp_f32_e32 v32, v32\n\t"
  "v_exp_f32_e32 v33, v33\n\t"
  "v_smfmac_f32_32x32x32_bf16 v[0:15], v[116:119], v[24:31], v164\n\t"
  "v_exp_f32_e32 v34, v34\n\t"
  "v_exp_f32_e32 v35, v35\n\t"
  "v_exp_f32_e32 v36, v36\n\t"
  "v_mfma_f32_32x32x16_bf16 v[16:31], v[72:75], v[144:147], 0\n\t"
  "v_exp_f32_e32 v37, v37\n\t"
  "v_exp_f32_e32 v38, v38\n\t"
  "s_waitcnt lgkmcnt(0)\n\t"
  "v_cmp_eq_u32_e32 vcc, 0, v163\n\t"
  "s_cbranch_vccnz Ls2_%=\n\t"
  "Lr2_%=:\n\t"
  "ds_read_b128 v[148:151], v162\n\t"
  "ds_read_b128 v[152:155], v162 offset:16\n\t"
  "ds_read_b32 v156, v162 offset:32\n\t"
  "v_exp_f32_e32 v39, v39\n\t"
  "v_exp_f32_e32 v40, v40\n\t"
  "v_exp_f32_e32 v41, v41\n\t"
  "v_smfmac_f32_32x32x32_bf16 v[0:15], v[120:123], v[32:39], v164\n\t"
  "v_exp_f32_e32 v42, v42\n\t"
  "v_exp_f32_e32 v43, v43\n\t"
  "v_exp_f32_e32 v44, v44\n\t"
  "v_exp_f32_e32 v45, v45\n\t"
  "v_exp_f32_e32 v46, v46\n\t"
  "v_exp_f32_e32 v47, v47\n\t"
  "v_exp_f32_e32 v16, v16\n\t"
  "v_exp_f32_e32 v17, v17\n\t"
  "v_smfmac_f32_32x32x32_bf16 v[0:15], v[124:127], v[40:47], v164\n\t"
  "v_exp_f32_e32 v18, v18\n\t"
  "v_exp_f32_e32 v19, v19\n\t"
  "v_exp_f32_e32 v20, v20\n\t"
  "v_mfma_f32_32x32x16_bf16 v[32:47], v[76:79], v[144:147], 0\n\t"
  "ds_read_b128 v[144:147], v161\n\t"
  "v_exp_f32_e32 v21, v21\n\t"
  "v_exp_f32_e32 v22, v22\n\t"
  "v_exp_f32_e32 v23, v23\n\t"
  "v_exp_f32_e32 v24, v24\n\t"
  "v_exp_f32_e32 v25, v25\n\t"
  "v_smfmac_f32_32x32x32_bf16 v[0:15], v[128:131], v[16:23], v164\n\t"
  "v_exp_f32_e32 v26, v26\n\t"
  "v_exp_f32_e32 v27, v27\n\t"
  "v_exp_f32_e32 v28, v28\n\t"
  "v_exp_f32_e32 v29, v29\n\t"
  "v_exp_f32_e32 v30, v30\n\t"
  "v_exp_f32_e32 v31, v31\n\t"
  "v_exp_f32_e32 v32, v32\n\t"
  "v_exp_f32_e32 v33, v33\n\t"
  "v_smfmac_f32_32x32x32_bf16 v[0:15], v[132:135], v[24:31], v164\n\t"
  "v_exp_f32_e32 v34, v34\n\t"
  "v_exp_f32_e32 v35, v35\n\t"
  "v_exp_f32_e32 v36, v36\n\t"
  "s_waitcnt lgkmcnt(0)\n\t"
  "v_mfma_f32_32x32x16_bf16 v[16:31], v[48:51], v[144:147], 0\n\t"
  "v_exp_f32_e32 v37, v37\n\t"
  "v_exp_f32_e32 v38, v38\n\t"
  "v_exp_f32_e32 v39, v39\n\t"
  "v_exp_f32_e32 v40, v40\n\t"
  "v_exp_f32_e32 v41, v41\n\t"
  "v_smfmac_f32_32x32x32_bf16 v[0:15], v[136:139], v[32:39], v164\n\t"
  "v_exp_f32_e32 v42, v42\n\t"
  "v_exp_f32_e32 v43, v43\n\t"
  "v_exp_f32_e32 v44, v44\n\t"
  "v_exp_f32_e32 v45, v45\n\t"
  "v_exp_f32_e32 v46, v46\n\t"
  "v_exp_f32_e32 v47, v47\n\t"
  "v_exp_f32_e32 v16, v16\n\t"
  "v_exp_f32_e32 v17, v17\n\t"
  "v_smfmac_f32_32x32x32_bf16 v[0:15], v[140:143], v[40:47], v164\n\t"
  "v_exp_f32_e32 v18, v18\n\t"
  "v_exp_f32_e32 v19, v19\n\t"
  "v_exp_f32_e32 v20, v20\n\t"
  "v_mfma_f32_32x32x16_bf16 v[32:47], v[52:55], v[144:147], 0\n\t"
  "v_exp_f32_e32 v21, v21\n\t"
  "v_exp_f32_e32 v22, v22\n\t"
  "v_exp_f32_e32 v23, v23\n\t"
  "v_exp_f32_e32 v24, v24\n\t"
  "v_exp_f32_e32 v25, v25\n\t"
  "v_smfmac_f32_32x32x32_bf16 v[148:163], v[80:83], v[16:23], v164\n\t"
  "v_exp_f32_e32 v26, v26\n\t"
  "v_exp_f32_e32 v27, v27\n\t"
  "v_exp_f32_e32 v28, v28\n\t"
  "v_xor_b32_e32 v14, 0x2000, v165\n\t"
  "ds_write_b128 v14, v[0:3]\n\t"
  "ds_write_b128 v14, v[4:7] offset:16\n\t"
  "ds_write_b32 v14, v8 offset:32\n\t"
  "ds_write_b32 v14, v164 offset:40\n\t"
  "v_exp_f32_e32 v29, v29\n\t"
  "v_exp_f32_e32 v30, v30\n\t"
  "v_exp_f32_e32 v31, v31\n\t"
  "v_exp_f32_e32 v32, v32\n\t"
  "v_exp_f32_e32 v33, v33\n\t"
  "v_smfmac_f32_32x32x32_bf16 v[148:163], v[84:87], v[24:31], v164\n\t"
  "v_exp_f32_e32 v34, v34\n\t"
  "v_exp_f32_e32 v35, v35\n\t"
  "v_exp_f32_e32 v36, v36\n\t"
  "v_mfma_f32_32x32x16_bf16 v[16:31], v[56:59], v[144:147], 0\n\t"
  "v_exp_f32_e32 v37, v37\n\t"
  "v_exp_f32_e32 v38, v38\n\t"
  "v_exp_f32_e32 v39, v39\n\t"
  "v_exp_f32_e32 v40, v40\n\t"
  "v_exp_f32_e32 v41, v41\n\t"
  "v_smfmac_f32_32x32x32_bf16 v[148:163], v[88:91], v[32:39], v164\n\t"
  "v_exp_f32_e32 v42, v42\n\t"
  "v_exp_f32_e32 v43, v43\n\t"
  "v_exp_f32_e32 v44, v44\n\t"
  "v_exp_f32_e32 v45, v45\n\t"
  "v_exp_f32_e32 v46, v46\n\t"
  "v_exp_f32_e32 v47, v47\n\t"
  "v_exp_f32_e32 v16, v16\n\t"
  "v_exp_f32_e32 v17, v17\n\t"
  "v_smfmac_f32_32x32x32_bf16 v[148:163], v[92:95], v[40:47], v164\n\t"
  "v_exp_f32_e32 v18, v18\n\t"
  "v_exp_f32_e32 v19, v19\n\t"
  "v_exp_f32_e32 v20, v20\n\t"
  "v_mfma_f32_32x32x16_bf16 v[32:47], v[60:63], v[144:147], 0\n\t"
  "v_exp_f32_e32 v21, v21\n\t"
  "v_exp_f32_e32 v22, v22\n\t"
  "v_exp_f32_e32 v23, v23\n\t"
  "v_exp_f32_e32 v24, v24\n\t"
  "v_exp_f32_e32 v25, v25\n\t"
  "v_smfmac_f32_32x32x32_bf16 v[148:163], v[96:99], v[16:23], v164\n\t"
  "v_exp_f32_e32 v26, v26\n\t"
  "v_exp_f32_e32 v27, v27\n\t"
  "v_exp_f32_e32 v28, v28\n\t"
  "v_exp_f32_e32 v29, v29\n\t"
  "v_exp_f32_e32 v30, v30\n\t"
  "v_exp_f32_e32 v31, v31\n\t"
  "v_exp_f32_e32 v32, v32\n\t"
  "v_exp_f32_e32 v33, v33\n\t"
  "v_smfmac_f32_32x32x32_bf16 v[148:163], v[100:103], v[24:31], v164\n\t"
  "v_exp_f32_e32 v34, v34\n\t"
  "v_exp_f32_e32 v35, v35\n\t"
  "v_exp_f32_e32 v36, v36\n\t"
  "v_mfma_f32_32x32x16_bf16 v[16:31], v[64:67], v[144:147], 0\n\t"
  "v_exp_f32_e32 v37, v37\n\t"
  "v_exp_f32_e32 v38, v38\n\t"
  "v_xor_b32_e32 v13, 0x4000, v166\n\t"
  "v_exp_f32_e32 v39, v39\n\t"
  "v_exp_f32_e32 v40, v40\n\t"
  "v_exp_f32_e32 v41, v41\n\t"
  "v_smfmac_f32_32x32x32_bf16 v[148:163], v[104:107], v[32:39], v164\n\t"
  "v_exp_f32_e32 v42, v42\n\t"
  "v_exp_f32_e32 v43, v43\n\t"
  "v_exp_f32_e32 v44, v44\n\t"
  "v_exp_f32_e32 v45, v45\n\t"
  "v_exp_f32_e32 v46, v46\n\t"
  "v_exp_f32_e32 v47, v47\n\t"
  "v_exp_f32_e32 v16, v16\n\t"
  "v_exp_f32_e32 v17, v17\n\t"
  "v_smfmac_f32_32x32x32_bf16 v[148:163], v[108:111], v[40:47], v164\n\t"
  "v_exp_f32_e32 v18, v18\n\t"
  "v_exp_f32_e32 v19, v19\n\t"
  "v_exp_f32_e32 v20, v20\n\t"
  "v_mfma_f32_32x32x16_bf16 v[32:47], v[68:71], v[144:147], 0\n\t"
  "v_exp_f32_e32 v21, v21\n\t"
  "v_exp_f32_e32 v22, v22\n\t"
  "v_xor_b32_e32 v14, 0x4000, v165\n\t"
  "ds_read_b32 v15, v14 offset:40\n\t"
  "v_exp_f32_e32 v23, v23\n\t"
  "v_exp_f32_e32 v24, v24\n\t"
  "v_exp_f32_e32 v25, v25\n\t"
  "v_smfmac_f32_32x32x32_bf16 v[148:163], v[112:115], v[16:23], v164\n\t"
  "v_exp_f32_e32 v26, v26\n\t"
  "v_exp_f32_e32 v27, v27\n\t"
  "v_exp_f32_e32 v28, v28\n\t"
  "v_exp_f32_e32 v29, v29\n\t"
  "v_exp_f32_e32 v30, v30\n\t"
  "v_exp_f32_e32 v31, v31\n\t"
  "v_exp_f32_e32 v32, v32\n\t"
  "v_exp_f32_e32 v33, v33\n\t"
  "v_smfmac_f32_32x32x32_bf16 v[148:163], v[116:119], v[24:31], v164\n\t"
  "v_exp_f32_e32 v34, v34\n\t"
  "v_exp_f32_e32 v35, v35\n\t"
  "v_exp_f32_e32 v36, v36\n\t"
  "v_mfma_f32_32x32x16_bf16 v[16:31], v[72:75], v[144:147], 0\n\t"
  "v_exp_f32_e32 v37, v37\n\t"
  "v_exp_f32_e32 v38, v38\n\t"
  "s_waitcnt lgkmcnt(0)\n\t"
  "v_cmp_eq_u32_e32 vcc, 0, v15\n\t"
  "s_cbranch_vccnz Ls3_%=\n\t"
  "Lr3_%=:\n\t"
  "ds_read_b128 v[0:3], v14\n\t"
  "ds_read_b128 v[4:7], v14 offset:16\n\t"
  "ds_read_b32 v8, v14 offset:32\n\t"
  "v_exp_f32_e32 v39, v39\n\t"
  "v_exp_f32_e32 v40, v40\n\t"
  "v_exp_f32_e32 v41, v41\n\t"
  "v_smfmac_f32_32x32x32_bf16 v[148:163], v[120:123], v[32:39], v164\n\t"
  "v_exp_f32_e32 v42, v42\n\t"
  "v_exp_f32_e32 v43, v43\n\t"
  "v_exp_f32_e32 v44, v44\n\t"
  "v_exp_f32_e32 v45, v45\n\t"
  "v_exp_f32_e32 v46, v46\n\t"
  "v_exp_f32_e32 v47, v47\n\t"
  "v_exp_f32_e32 v16, v16\n\t"
  "v_exp_f32_e32 v17, v17\n\t"
  "v_smfmac_f32_32x32x32_bf16 v[148:163], v[124:127], v[40:47], v164\n\t"
  "v_exp_f32_e32 v18, v18\n\t"
  "v_exp_f32_e32 v19, v19\n\t"
  "v_exp_f32_e32 v20, v20\n\t"
  "v_mfma_f32_32x32x16_bf16 v[32:47], v[76:79], v[144:147], 0\n\t"
  "ds_read_b128 v[144:147], v13\n\t"
  "v_exp_f32_e32 v21, v21\n\t"
  "v_exp_f32_e32 v22, v22\n\t"
  "v_exp_f32_e32 v23, v23\n\t"
  "v_exp_f32_e32 v24, v24\n\t"
  "v_exp_f32_e32 v25, v25\n\t"
  "v_smfmac_f32_32x32x32_bf16 v[148:163], v[128:131], v[16:23], v164\n\t"
  "v_exp_f32_e32 v26, v26\n\t"
  "v_exp_f32_e32 v27, v27\n\t"
  "v_exp_f32_e32 v28, v28\n\t"
  "v_exp_f32_e32 v29, v29\n\t"
  "v_exp_f32_e32 v30, v30\n\t"
  "v_exp_f32_e32 v31, v31\n\t"
  "v_exp_f32_e32 v32, v32\n\t"
  "v_exp_f32_e32 v33, v33\n\t"
  "v_smfmac_f32_32x32x32_bf16 v[148:163], v[132:135], v[24:31], v164\n\t"
  "v_exp_f32_e32 v34, v34\n\t"
  "v_exp_f32_e32 v35, v35\n\t"
  "v_exp_f32_e32 v36, v36\n\t"
  "s_waitcnt lgkmcnt(0)\n\t"
  "v_mfma_f32_32x32x16_bf16 v[16:31], v[48:51], v[144:147], 0\n\t"
  "v_exp_f32_e32 v37, v37\n\t"
  "v_exp_f32_e32 v38, v38\n\t"
  "v_exp_f32_e32 v39, v39\n\t"
  "v_exp_f32_e32 v40, v40\n\t"
  "v_exp_f32_e32 v41, v41\n\t"
  "v_smfmac_f32_32x32x32_bf16 v[148:163], v[136:139], v[32:39], v164\n\t"
  "v_exp_f32_e32 v42, v42\n\t"
  "v_exp_f32_e32 v43, v43\n\t"
  "v_exp_f32_e32 v44, v44\n\t"
  "v_exp_f32_e32 v45, v45\n\t"
  "v_exp_f32_e32 v46, v46\n\t"
  "v_exp_f32_e32 v47, v47\n\t"
  "v_exp_f32_e32 v16, v16\n\t"
  "v_exp_f32_e32 v17, v17\n\t"
  "v_smfmac_f32_32x32x32_bf16 v[148:163], v[140:143], v[40:47], v164\n\t"
  "v_exp_f32_e32 v18, v18\n\t"
  "v_exp_f32_e32 v19, v19\n\t"
  "v_exp_f32_e32 v20, v20\n\t"
  "v_mfma_f32_32x32x16_bf16 v[32:47], v[52:55], v[144:147], 0\n\t"
  "v_exp_f32_e32 v21, v21\n\t"
  "v_exp_f32_e32 v22, v22\n\t"
  "v_exp_f32_e32 v23, v23\n\t"
  "v_exp_f32_e32 v24, v24\n\t"
  "v_exp_f32_e32 v25, v25\n\t"
  "v_smfmac_f32_32x32x32_bf16 v[0:15], v[80:83], v[16:23], v164\n\t"
  "v_exp_f32_e32 v26, v26\n\t"
  "v_exp_f32_e32 v27, v27\n\t"
  "v_exp_f32_e32 v28, v28\n\t"
  "v_xor_b32_e32 v162, 0x3000, v165\n\t"
  "ds_write_b128 v162, v[148:151]\n\t"
  "ds_write_b128 v162, v[152:155] offset:16\n\t"
  "ds_write_b32 v162, v156 offset:32\n\t"
  "ds_write_b32 v162, v164 offset:40\n\t"
  "v_exp_f32_e32 v29, v29\n\t"
  "v_exp_f32_e32 v30, v30\n\t"
  "v_exp_f32_e32 v31, v31\n\t"
  "v_exp_f32_e32 v32, v32\n\t"
  "v_exp_f32_e32 v33, v33\n\t"
  "v_smfmac_f32_32x32x32_bf16 v[0:15], v[84:87], v[24:31], v164\n\t"
  "v_exp_f32_e32 v34, v34\n\t"
  "v_exp_f32_e32 v35, v35\n\t"
  "v_exp_f32_e32 v36, v36\n\t"
  "v_mfma_f32_32x32x16_bf16 v[16:31], v[56:59], v[144:147], 0\n\t"
  "v_exp_f32_e32 v37, v37\n\t"
  "v_exp_f32_e32 v38, v38\n\t"
  "v_exp_f32_e32 v39, v39\n\t"
  "v_exp_f32_e32 v40, v40\n\t"
  "v_exp_f32_e32 v41, v41\n\t"
  "v_smfmac_f32_32x32x32_bf16 v[0:15], v[88:91], v[32:39], v164\n\t"
  "v_exp_f32_e32 v42, v42\n\t"
  "v_exp_f32_e32 v43, v43\n\t"
  "v_exp_f32_e32 v44, v44\n\t"
  "v_exp_f32_e32 v45, v45\n\t"
  "v_exp_f32_e32 v46, v46\n\t"
  "v_exp_f32_e32 v47, v47\n\t"
  "v_exp_f32_e32 v16, v16\n\t"
  "v_exp_f32_e32 v17, v17\n\t"
  "v_smfmac_f32_32x32x32_bf16 v[0:15], v[92:95], v[40:47], v164\n\t"
  "v_exp_f32_e32 v18, v18\n\t"
  "v_exp_f32_e32 v19, v19\n\t"
  "v_exp_f32_e32 v20, v20\n\t"
  "v_mfma_f32_32x32x16_bf16 v[32:47], v[60:63], v[144:147], 0\n\t"
  "v_exp_f32_e32 v21, v21\n\t"
  "v_exp_f32_e32 v22, v22\n\t"
  "v_exp_f32_e32 v23, v23\n\t"
  "v_exp_f32_e32 v24, v24\n\t"
  "v_exp_f32_e32 v25, v25\n\t"
  "v_smfmac_f32_32x32x32_bf16 v[0:15], v[96:99], v[16:23], v164\n\t"
  "v_exp_f32_e32 v26, v26\n\t"
  "v_exp_f32_e32 v27, v27\n\t"
  "v_exp_f32_e32 v28, v28\n\t"
  "v_exp_f32_e32 v29, v29\n\t"
  "v_exp_f32_e32 v30, v30\n\t"
  "v_exp_f32_e32 v31, v31\n\t"
  "v_exp_f32_e32 v32, v32\n\t"
  "v_exp_f32_e32 v33, v33\n\t"
  "v_smfmac_f32_32x32x32_bf16 v[0:15], v[100:103], v[24:31], v164\n\t"
  "v_exp_f32_e32 v34, v34\n\t"
  "v_exp_f32_e32 v35, v35\n\t"
  "v_exp_f32_e32 v36, v36\n\t"
  "v_mfma_f32_32x32x16_bf16 v[16:31], v[64:67], v[144:147], 0\n\t"
  "v_exp_f32_e32 v37, v37\n\t"
  "v_exp_f32_e32 v38, v38\n\t"
  "v_xor_b32_e32 v161, 0x5000, v166\n\t"
  "v_exp_f32_e32 v39, v39\n\t"
  "v_exp_f32_e32 v40, v40\n\t"
  "v_exp_f32_e32 v41, v41\n\t"
  "v_smfmac_f32_32x32x32_bf16 v[0:15], v[104:107], v[32:39], v164\n\t"
  "v_exp_f32_e32 v42, v42\n\t"
  "v_exp_f32_e32 v43, v43\n\t"
  "v_exp_f32_e32 v44, v44\n\t"
  "v_exp_f32_e32 v45, v45\n\t"
  "v_exp_f32_e32 v46, v46\n\t"
  "v_exp_f32_e32 v47, v47\n\t"
  "v_exp_f32_e32 v16, v16\n\t"
  "v_exp_f32_e32 v17, v17\n\t"
  "v_smfmac_f32_32x32x32_bf16 v[0:15], v[108:111], v[40:47], v164\n\t"
  "v_exp_f32_e32 v18, v18\n\t"
  "v_exp_f32_e32 v19, v19\n\t"
  "v_exp_f32_e32 v20, v20\n\t"
  "v_mfma_f32_32x32x16_bf16 v[32:47], v[68:71], v[144:147], 0\n\t"
  "v_exp_f32_e32 v21, v21\n\t"
  "v_exp_f32_e32 v22, v22\n\t"
  "v_xor_b32_e32 v162, 0x5000, v165\n\t"
  "ds_read_b32 v163, v162 offset:40\n\t"
  "v_exp_f32_e32 v23, v23\n\t"
  "v_exp_f32_e32 v24, v24\n\t"
  "v_exp_f32_e32 v25, v25\n\t"
  "v_smfmac_f32_32x32x32_bf16 v[0:15], v[112:115], v[16:23], v164\n\t"
  "v_exp_f32_e32 v26, v26\n\t"
  "v_exp_f32_e32 v27, v27\n\t"
  "v_exp_f32_e32 v28, v28\n\t"
  "v_exp_f32_e32 v29, v29\n\t"
  "v_exp_f32_e32 v30, v30\n\t"
  "v_exp_f32_e32 v31, v31\n\t"
  "v_exp_f32_e32 v32, v32\n\t"
  "v_exp_f32_e32 v33, v33\n\t"
  "v_smfmac_f32_32x32x32_bf16 v[0:15], v[116:119], v[24:31], v164\n\t"
  "v_exp_f32_e32 v34, v34\n\t"
  "v_exp_f32_e32 v35, v35\n\t"
  "v_exp_f32_e32 v36, v36\n\t"
  "v_mfma_f32_32x32x16_bf16 v[16:31], v[72:75], v[144:147], 0\n\t"
  "v_exp_f32_e32 v37, v37\n\t"
  "v_exp_f32_e32 v38, v38\n\t"
  "s_waitcnt lgkmcnt(0)\n\t"
  "v_cmp_eq_u32_e32 vcc, 0, v163\n\t"
  "s_cbranch_vccnz Ls4_%=\n\t"
  "Lr4_%=:\n\t"
  "ds_read_b128 v[148:151], v162\n\t"
  "ds_read_b128 v[152:155], v162 offset:16\n\t"
  "ds_read_b32 v156, v162 offset:32\n\t"
  "v_exp_f32_e32 v39, v39\n\t"
  "v_exp_f32_e32 v40, v40\n\t"
  "v_exp_f32_e32 v41, v41\n\t"
  "v_smfmac_f32_32x32x32_bf16 v[0:15], v[120:123], v[32:39], v164\n\t"
  "v_exp_f32_e32 v42, v42\n\t"
  "v_exp_f32_e32 v43, v43\n\t"
  "v_exp_f32_e32 v44, v44\n\t"
  "v_exp_f32_e32 v45, v45\n\t"
  "v_exp_f32_e32 v46, v46\n\t"
  "v_exp_f32_e32 v47, v47\n\t"
  "v_exp_f32_e32 v16, v16\n\t"
  "v_exp_f32_e32 v17, v17\n\t"
  "v_smfmac_f32_32x32x32_bf16 v[0:15], v[124:127], v[40:47], v164\n\t"
  "v_exp_f32_e32 v18, v18\n\t"
  "v_exp_f32_e32 v19, v19\n\t"
  "v_exp_f32_e32 v20, v20\n\t"
  "v_mfma_f32_32x32x16_bf16 v[32:47], v[76:79], v[144:147], 0\n\t"
  "ds_read_b128 v[144:147], v161\n\t"
  "v_exp_f32_e32 v21, v21\n\t"
  "v_exp_f32_e32 v22, v22\n\t"
  "v_exp_f32_e32 v23, v23\n\t"
  "v_exp_f32_e32 v24, v24\n\t"
  "v_exp_f32_e32 v25, v25\n\t"
  "v_smfmac_f32_32x32x32_bf16 v[0:15], v[128:131], v[16:23], v164\n\t"
  "v_exp_f32_e32 v26, v26\n\t"
  "v_exp_f32_e32 v27, v27\n\t"
  "v_exp_f32_e32 v28, v28\n\t"
  "v_exp_f32_e32 v29, v29\n\t"
  "v_exp_f32_e32 v30, v30\n\t"
  "v_exp_f32_e32 v31, v31\n\t"
  "v_exp_f32_e32 v32, v32\n\t"
  "v_exp_f32_e32 v33, v33\n\t"
  "v_smfmac_f32_32x32x32_bf16 v[0:15], v[132:135], v[24:31], v164\n\t"
  "v_exp_f32_e32 v34, v34\n\t"
  "v_exp_f32_e32 v35, v35\n\t"
  "v_exp_f32_e32 v36, v36\n\t"
  "s_waitcnt lgkmcnt(0)\n\t"
  "v_mfma_f32_32x32x16_bf16 v[16:31], v[48:51], v[144:147], 0\n\t"
  "v_exp_f32_e32 v37, v37\n\t"
  "v_exp_f32_e32 v38, v38\n\t"
  "v_exp_f32_e32 v39, v39\n\t"
  "v_exp_f32_e32 v40, v40\n\t"
  "v_exp_f32_e32 v41, v41\n\t"
  "v_smfmac_f32_32x32x32_bf16 v[0:15], v[136:139], v[32:39], v164\n\t"
  "v_exp_f32_e32 v42, v42\n\t"
  "v_exp_f32_e32 v43, v43\n\t"
  "v_exp_f32_e32 v44, v44\n\t"
  "v_exp_f32_e32 v45, v45\n\t"
  "v_exp_f32_e32 v46, v46\n\t"
  "v_exp_f32_e32 v47, v47\n\t"
  "v_exp_f32_e32 v16, v16\n\t"
  "v_exp_f32_e32 v17, v17\n\t"
  "v_smfmac_f32_32x32x32_bf16 v[0:15], v[140:143], v[40:47], v164\n\t"
  "v_exp_f32_e32 v18, v18\n\t"
  "v_exp_f32_e32 v19, v19\n\t"
  "v_exp_f32_e32 v20, v20\n\t"
  "v_mfma_f32_32x32x16_bf16 v[32:47], v[52:55], v[144:147], 0\n\t"
  "v_exp_f32_e32 v21, v21\n\t"
  "v_exp_f32_e32 v22, v22\n\t"
  "v_exp_f32_e32 v23, v23\n\t"
  "v_exp_f32_e32 v24, v24\n\t"
  "v_exp_f32_e32 v25, v25\n\t"
  "v_smfmac_f32_32x32x32_bf16 v[148:163], v[80:83], v[16:23], v164\n\t"
  "v_exp_f32_e32 v26, v26\n\t"
  "v_exp_f32_e32 v27, v27\n\t"
  "v_exp_f32_e32 v28, v28\n\t"
  "v_xor_b32_e32 v14, 0x4000, v165\n\t"
  "ds_write_b128 v14, v[0:3]\n\t"
  "ds_write_b128 v14, v[4:7] offset:16\n\t"
  "ds_write_b32 v14, v8 offset:32\n\t"
  "ds_write_b32 v14, v164 offset:44\n\t"
  "v_exp_f32_e32 v29, v29\n\t"
  "v_exp_f32_e32 v30, v30\n\t"
  "v_exp_f32_e32 v31, v31\n\t"
  "v_exp_f32_e32 v32, v32\n\t"
  "v_exp_f32_e32 v33, v33\n\t"
  "v_smfmac_f32_32x32x32_bf16 v[148:163], v[84:87], v[24:31], v164\n\t"
  "v_exp_f32_e32 v34, v34\n\t"
  "v_exp_f32_e32 v35, v35\n\t"
  "v_exp_f32_e32 v36, v36\n\t"
  "v_mfma_f32_32x32x16_bf16 v[16:31], v[56:59], v[144:147], 0\n\t"
  "v_exp_f32_e32 v37, v37\n\t"
  "v_exp_f32_e32 v38, v38\n\t"
  "v_exp_f32_e32 v39, v39\n\t"
  "v_exp_f32_e32 v40, v40\n\t"
  "v_exp_f32_e32 v41, v41\n\t"
  "v_smfmac_f32_32x32x32_bf16 v[148:163], v[88:91], v[32:39], v164\n\t"
  "v_exp_f32_e32 v42, v42\n\t"
  "v_exp_f32_e32 v43, v43\n\t"
  "v_exp_f32_e32 v44, v44\n\t"
  "v_exp_f32_e32 v45, v45\n\t"
  "v_exp_f32_e32 v46, v46\n\t"
  "v_exp_f32_e32 v47, v47\n\t"
  "v_exp_f32_e32 v16, v16\n\t"
  "v_exp_f32_e32 v17, v17\n\t"
  "v_smfmac_f32_32x32x32_bf16 v[148:163], v[92:95], v[40:47], v164\n\t"
  "v_exp_f32_e32 v18, v18\n\t"
  "v_exp_f32_e32 v19, v19\n\t"
  "v_exp_f32_e32 v20, v20\n\t"
  "v_mfma_f32_32x32x16_bf16 v[32:47], v[60:63], v[144:147], 0\n\t"
  "v_exp_f32_e32 v21, v21\n\t"
  "v_exp_f32_e32 v22, v22\n\t"
  "v_exp_f32_e32 v23, v23\n\t"
  "v_exp_f32_e32 v24, v24\n\t"
  "v_exp_f32_e32 v25, v25\n\t"
  "v_smfmac_f32_32x32x32_bf16 v[148:163], v[96:99], v[16:23], v164\n\t"
  "v_exp_f32_e32 v26, v26\n\t"
  "v_exp_f32_e32 v27, v27\n\t"
  "v_exp_f32_e32 v28, v28\n\t"
  "v_exp_f32_e32 v29, v29\n\t"
  "v_exp_f32_e32 v30, v30\n\t"
  "v_exp_f32_e32 v31, v31\n\t"
  "v_exp_f32_e32 v32, v32\n\t"
  "v_exp_f32_e32 v33, v33\n\t"
  "v_smfmac_f32_32x32x32_bf16 v[148:163], v[100:103], v[24:31], v164\n\t"
  "v_exp_f32_e32 v34, v34\n\t"
  "v_exp_f32_e32 v35, v35\n\t"
  "v_exp_f32_e32 v36, v36\n\t"
  "v_mfma_f32_32x32x16_bf16 v[16:31], v[64:67], v[144:147], 0\n\t"
  "v_exp_f32_e32 v37, v37\n\t"
  "v_exp_f32_e32 v38, v38\n\t"
  "v_xor_b32_e32 v13, 0x6000, v166\n\t"
  "v_exp_f32_e32 v39, v39\n\t"
  "v_exp_f32_e32 v40, v40\n\t"
  "v_exp_f32_e32 v41, v41\n\t"
  "v_smfmac_f32_32x32x32_bf16 v[148:163], v[104:107], v[32:39], v164\n\t"
  "v_exp_f32_e32 v42, v42\n\t"
  "v_exp_f32_e32 v43, v43\n\t"
  "v_exp_f32_e32 v44, v44\n\t"
  "v_exp_f32_e32 v45, v45\n\t"
  "v_exp_f32_e32 v46, v46\n\t"
  "v_exp_f32_e32 v47, v47\n\t"
  "v_exp_f32_e32 v16, v16\n\t"
  "v_exp_f32_e32 v17, v17\n\t"
  "v_smfmac_f32_32x32x32_bf16 v[148:163], v[108:111], v[40:47], v164\n\t"
  "v_exp_f32_e32 v18, v18\n\t"
  "v_exp_f32_e32 v19, v19\n\t"
  "v_exp_f32_e32 v20, v20\n\t"
  "v_mfma_f32_32x32x16_bf16 v[32:47], v[68:71], v[144:147], 0\n\t"
  "v_exp_f32_e32 v21, v21\n\t"
  "v_exp_f32_e32 v22, v22\n\t"
  "v_xor_b32_e32 v14, 0x6000, v165\n\t"
  "ds_read_b32 v15, v14 offset:44\n\t"
  "v_exp_f32_e32 v23, v23\n\t"
  "v_exp_f32_e32 v24, v24\n\t"
  "v_exp_f32_e32 v25, v25\n\t"
  "v_smfmac_f32_32x32x32_bf16 v[148:163], v[112:115], v[16:23], v164\n\t"
  "v_exp_f32_e32 v26, v26\n\t"
  "v_exp_f32_e32 v27, v27\n\t"
  "v_exp_f32_e32 v28, v28\n\t"
  "v_exp_f32_e32 v29, v29\n\t"
  "v_exp_f32_e32 v30, v30\n\t"
  "v_exp_f32_e32 v31, v31\n\t"
  "v_exp_f32_e32 v32, v32\n\t"
  "v_exp_f32_e32 v33, v33\n\t"
  "v_smfmac_f32_32x32x32_bf16 v[148:163], v[116:119], v[24:31], v164\n\t"
  "v_exp_f32_e32 v34, v34\n\t"
  "v_exp_f32_e32 v35, v35\n\t"
  "v_exp_f32_e32 v36, v36\n\t"
  "v_mfma_f32_32x32x16_bf16 v[16:31], v[72:75], v[144:147], 0\n\t"
  "v_exp_f32_e32 v37, v37\n\t"
  "v_exp_f32_e32 v38, v38\n\t"
  "s_waitcnt lgkmcnt(0)\n\t"
  "v_cmp_eq_u32_e32 vcc, 0, v15\n\t"
  "s_cbranch_vccnz Ls5_%=\n\t"
  "Lr5_%=:\n\t"
  "ds_read_b128 v[0:3], v14\n\t"
  "ds_read_b128 v[4:7], v14 offset:16\n\t"
  "ds_read_b32 v8, v14 offset:32\n\t"
  "v_exp_f32_e32 v39, v39\n\t"
  "v_exp_f32_e32 v40, v40\n\t"
  "v_exp_f32_e32 v41, v41\n\t"
  "v_smfmac_f32_32x32x32_bf16 v[148:163], v[120:123], v[32:39], v164\n\t"
  "v_exp_f32_e32 v42, v42\n\t"
  "v_exp_f32_e32 v43, v43\n\t"
  "v_exp_f32_e32 v44, v44\n\t"
  "v_exp_f32_e32 v45, v45\n\t"
  "v_exp_f32_e32 v46, v46\n\t"
  "v_exp_f32_e32 v47, v47\n\t"
  "v_exp_f32_e32 v16, v16\n\t"
  "v_exp_f32_e32 v17, v17\n\t"
  "v_smfmac_f32_32x32x32_bf16 v[148:163], v[124:127], v[40:47], v164\n\t"
  "v_exp_f32_e32 v18, v18\n\t"
  "v_exp_f32_e32 v19, v19\n\t"
  "v_exp_f32_e32 v20, v20\n\t"
  "v_mfma_f32_32x32x16_bf16 v[32:47], v[76:79], v[144:147], 0\n\t"
  "ds_read_b128 v[144:147], v13\n\t"
  "v_exp_f32_e32 v21, v21\n\t"
  "v_exp_f32_e32 v22, v22\n\t"
  "v_exp_f32_e32 v23, v23\n\t"
  "v_exp_f32_e32 v24, v24\n\t"
  "v_exp_f32_e32 v25, v25\n\t"
  "v_smfmac_f32_32x32x32_bf16 v[148:163], v[128:131], v[16:23], v164\n\t"
  "v_exp_f32_e32 v26, v26\n\t"
  "v_exp_f32_e32 v27, v27\n\t"
  "v_exp_f32_e32 v28, v28\n\t"
  "v_exp_f32_e32 v29, v29\n\t"
  "v_exp_f32_e32 v30, v30\n\t"
  "v_exp_f32_e32 v31, v31\n\t"
  "v_exp_f32_e32 v32, v32\n\t"
  "v_exp_f32_e32 v33, v33\n\t"
  "v_smfmac_f32_32x32x32_bf16 v[148:163], v[132:135], v[24:31], v164\n\t"
  "v_exp_f32_e32 v34, v34\n\t"
  "v_exp_f32_e32 v35, v35\n\t"
  "v_exp_f32_e32 v36, v36\n\t"
  "s_waitcnt lgkmcnt(0)\n\t"
  "v_mfma_f32_32x32x16_bf16 v[16:31], v[48:51], v[144:147], 0\n\t"
  "v_exp_f32_e32 v37, v37\n\t"
  "v_exp_f32_e32 v38, v38\n\t"
  "v_exp_f32_e32 v39, v39\n\t"
  "v_exp_f32_e32 v40, v40\n\t"
  "v_exp_f32_e32 v41, v41\n\t"
  "v_smfmac_f32_32x32x32_bf16 v[148:163], v[136:139], v[32:39], v164\n\t"
  "v_exp_f32_e32 v42, v42\n\t"
  "v_exp_f32_e32 v43, v43\n\t"
  "v_exp_f32_e32 v44, v44\n\t"
  "v_exp_f32_e32 v45, v45\n\t"
  "v_exp_f32_e32 v46, v46\n\t"
  "v_exp_f32_e32 v47, v47\n\t"
  "v_exp_f32_e32 v16, v16\n\t"
  "v_exp_f32_e32 v17, v17\n\t"
  "v_smfmac_f32_32x32x32_bf16 v[148:163], v[140:143], v[40:47], v164\n\t"
  "v_exp_f32_e32 v18, v18\n\t"
  "v_exp_f32_e32 v19, v19\n\t"
  "v_exp_f32_e32 v20, v20\n\t"
  "v_mfma_f32_32x32x16_bf16 v[32:47], v[52:55], v[144:147], 0\n\t"
  "v_exp_f32_e32 v21, v21\n\t"
  "v_exp_f32_e32 v22, v22\n\t"
  "v_exp_f32_e32 v23, v23\n\t"
  "v_exp_f32_e32 v24, v24\n\t"
  "v_exp_f32_e32 v25, v25\n\t"
  "v_smfmac_f32_32x32x32_bf16 v[0:15], v[80:83], v[16:23], v164\n\t"
  "v_exp_f32_e32 v26, v26\n\t"
  "v_exp_f32_e32 v27, v27\n\t"
  "v_exp_f32_e32 v28, v28\n\t"
  "v_xor_b32_e32 v162, 0x5000, v165\n\t"
  "ds_write_b128 v162, v[148:151]\n\t"
  "ds_write_b128 v162, v[152:155] offset:16\n\t"
  "ds_write_b32 v162, v156 offset:32\n\t"
  "ds_write_b32 v162, v164 offset:44\n\t"
  "v_exp_f32_e32 v29, v29\n\t"
  "v_exp_f32_e32 v30, v30\n\t"
  "v_exp_f32_e32 v31, v31\n\t"
  "v_exp_f32_e32 v32, v32\n\t"
  "v_exp_f32_e32 v33, v33\n\t"
  "v_smfmac_f32_32x32x32_bf16 v[0:15], v[84:87], v[24:31], v164\n\t"
  "v_exp_f32_e32 v34, v34\n\t"
  "v_exp_f32_e32 v35, v35\n\t"
  "v_exp_f32_e32 v36, v36\n\t"
  "v_mfma_f32_32x32x16_bf16 v[16:31], v[56:59], v[144:147], 0\n\t"
  "v_exp_f32_e32 v37, v37\n\t"
  "v_exp_f32_e32 v38, v38\n\t"
  "v_exp_f32_e32 v39, v39\n\t"
  "v_exp_f32_e32 v40, v40\n\t"
  "v_exp_f32_e32 v41, v41\n\t"
  "v_smfmac_f32_32x32x32_bf16 v[0:15], v[88:91], v[32:39], v164\n\t"
  "v_exp_f32_e32 v42, v42\n\t"
  "v_exp_f32_e32 v43, v43\n\t"
  "v_exp_f32_e32 v44, v44\n\t"
  "v_exp_f32_e32 v45, v45\n\t"
  "v_exp_f32_e32 v46, v46\n\t"
  "v_exp_f32_e32 v47, v47\n\t"
  "v_exp_f32_e32 v16, v16\n\t"
  "v_exp_f32_e32 v17, v17\n\t"
  "v_smfmac_f32_32x32x32_bf16 v[0:15], v[92:95], v[40:47], v164\n\t"
  "v_exp_f32_e32 v18, v18\n\t"
  "v_exp_f32_e32 v19, v19\n\t"
  "v_exp_f32_e32 v20, v20\n\t"
  "v_mfma_f32_32x32x16_bf16 v[32:47], v[60:63], v[144:147], 0\n\t"
  "v_exp_f32_e32 v21, v21\n\t"
  "v_exp_f32_e32 v22, v22\n\t"
  "v_exp_f32_e32 v23, v23\n\t"
  "v_exp_f32_e32 v24, v24\n\t"
  "v_exp_f32_e32 v25, v25\n\t"
  "v_smfmac_f32_32x32x32_bf16 v[0:15], v[96:99], v[16:23], v164\n\t"
  "v_exp_f32_e32 v26, v26\n\t"
  "v_exp_f32_e32 v27, v27\n\t"
  "v_exp_f32_e32 v28, v28\n\t"
  "v_exp_f32_e32 v29, v29\n\t"
  "v_exp_f32_e32 v30, v30\n\t"
  "v_exp_f32_e32 v31, v31\n\t"
  "v_exp_f32_e32 v32, v32\n\t"
  "v_exp_f32_e32 v33, v33\n\t"
  "v_smfmac_f32_32x32x32_bf16 v[0:15], v[100:103], v[24:31], v164\n\t"
  "v_exp_f32_e32 v34, v34\n\t"
  "v_exp_f32_e32 v35, v35\n\t"
  "v_exp_f32_e32 v36, v36\n\t"
  "v_mfma_f32_32x32x16_bf16 v[16:31], v[64:67], v[144:147], 0\n\t"
  "v_exp_f32_e32 v37, v37\n\t"
  "v_exp_f32_e32 v38, v38\n\t"
  "v_xor_b32_e32 v161, 0x7000, v166\n\t"
  "v_exp_f32_e32 v39, v39\n\t"
  "v_exp_f32_e32 v40, v40\n\t"
  "v_exp_f32_e32 v41, v41\n\t"
  "v_smfmac_f32_32x32x32_bf16 v[0:15], v[104:107], v[32:39], v164\n\t"
  "v_exp_f32_e32 v42, v42\n\t"
  "v_exp_f32_e32 v43, v43\n\t"
  "v_exp_f32_e32 v44, v44\n\t"
  "v_exp_f32_e32 v45, v45\n\t"
  "v_exp_f32_e32 v46, v46\n\t"
  "v_exp_f32_e32 v47, v47\n\t"
  "v_exp_f32_e32 v16, v16\n\t"
  "v_exp_f32_e32 v17, v17\n\t"
  "v_smfmac_f32_32x32x32_bf16 v[0:15], v[108:111], v[40:47], v164\n\t"
  "v_exp_f32_e32 v18, v18\n\t"
  "v_exp_f32_e32 v19, v19\n\t"
  "v_exp_f32_e32 v20, v20\n\t"
  "v_mfma_f32_32x32x16_bf16 v[32:47], v[68:71], v[144:147], 0\n\t"
  "v_exp_f32_e32 v21, v21\n\t"
  "v_exp_f32_e32 v22, v22\n\t"
  "v_xor_b32_e32 v162, 0x7000, v165\n\t"
  "ds_read_b32 v163, v162 offset:44\n\t"
  "v_exp_f32_e32 v23, v23\n\t"
  "v_exp_f32_e32 v24, v24\n\t"
  "v_exp_f32_e32 v25, v25\n\t"
  "v_smfmac_f32_32x32x32_bf16 v[0:15], v[112:115], v[16:23], v164\n\t"
  "v_exp_f32_e32 v26, v26\n\t"
  "v_exp_f32_e32 v27, v27\n\t"
  "v_exp_f32_e32 v28, v28\n\t"
  "v_exp_f32_e32 v29, v29\n\t"
  "v_exp_f32_e32 v30, v30\n\t"
  "v_exp_f32_e32 v31, v31\n\t"
  "v_exp_f32_e32 v32, v32\n\t"
  "v_exp_f32_e32 v33, v33\n\t"
  "v_smfmac_f32_32x32x32_bf16 v[0:15], v[116:119], v[24:31], v164\n\t"
  "v_exp_f32_e32 v34, v34\n\t"
  "v_exp_f32_e32 v35, v35\n\t"
  "v_exp_f32_e32 v36, v36\n\t"
  "v_mfma_f32_32x32x16_bf16 v[16:31], v[72:75], v[144:147], 0\n\t"
  "v_exp_f32_e32 v37, v37\n\t"
  "v_exp_f32_e32 v38, v38\n\t"
  "s_waitcnt lgkmcnt(0)\n\t"
  "v_cmp_eq_u32_e32 vcc, 0, v163\n\t"
  "s_cbranch_vccnz Ls6_%=\n\t"
  "Lr6_%=:\n\t"
  "ds_read_b128 v[148:151], v162\n\t"
  "ds_read_b128 v[152:155], v162 offset:16\n\t"
  "ds_read_b32 v156, v162 offset:32\n\t"
  "v_exp_f32_e32 v39, v39\n\t"
  "v_exp_f32_e32 v40, v40\n\t"
  "v_exp_f32_e32 v41, v41\n\t"
  "v_smfmac_f32_32x32x32_bf16 v[0:15], v[120:123], v[32:39], v164\n\t"
  "v_exp_f32_e32 v42, v42\n\t"
  "v_exp_f32_e32 v43, v43\n\t"
  "v_exp_f32_e32 v44, v44\n\t"
  "v_exp_f32_e32 v45, v45\n\t"
  "v_exp_f32_e32 v46, v46\n\t"
  "v_exp_f32_e32 v47, v47\n\t"
  "v_exp_f32_e32 v16, v16\n\t"
  "v_exp_f32_e32 v17, v17\n\t"
  "v_smfmac_f32_32x32x32_bf16 v[0:15], v[124:127], v[40:47], v164\n\t"
  "v_exp_f32_e32 v18, v18\n\t"
  "v_exp_f32_e32 v19, v19\n\t"
  "v_exp_f32_e32 v20, v20\n\t"
  "v_mfma_f32_32x32x16_bf16 v[32:47], v[76:79], v[144:147], 0\n\t"
  "ds_read_b128 v[144:147], v161\n\t"
  "v_exp_f32_e32 v21, v21\n\t"
  "v_exp_f32_e32 v22, v22\n\t"
  "v_exp_f32_e32 v23, v23\n\t"
  "v_exp_f32_e32 v24, v24\n\t"
  "v_exp_f32_e32 v25, v25\n\t"
  "v_smfmac_f32_32x32x32_bf16 v[0:15], v[128:131], v[16:23], v164\n\t"
  "v_exp_f32_e32 v26, v26\n\t"
  "v_exp_f32_e32 v27, v27\n\t"
  "v_exp_f32_e32 v28, v28\n\t"
  "v_exp_f32_e32 v29, v29\n\t"
  "v_exp_f32_e32 v30, v30\n\t"
  "v_exp_f32_e32 v31, v31\n\t"
  "v_exp_f32_e32 v32, v32\n\t"
  "v_exp_f32_e32 v33, v33\n\t"
  "v_smfmac_f32_32x32x32_bf16 v[0:15], v[132:135], v[24:31], v164\n\t"
  "v_exp_f32_e32 v34, v34\n\t"
  "v_exp_f32_e32 v35, v35\n\t"
  "v_exp_f32_e32 v36, v36\n\t"
  "s_waitcnt lgkmcnt(0)\n\t"
  "v_mfma_f32_32x32x16_bf16 v[16:31], v[48:51], v[144:147], 0\n\t"
  "v_exp_f32_e32 v37, v37\n\t"
  "v_exp_f32_e32 v38, v38\n\t"
  "v_exp_f32_e32 v39, v39\n\t"
  "v_exp_f32_e32 v40, v40\n\t"
  "v_exp_f32_e32 v41, v41\n\t"
  "v_smfmac_f32_32x32x32_bf16 v[0:15], v[136:139], v[32:39], v164\n\t"
  "v_exp_f32_e32 v42, v42\n\t"
  "v_exp_f32_e32 v43, v43\n\t"
  "v_exp_f32_e32 v44, v44\n\t"
  "v_exp_f32_e32 v45, v45\n\t"
  "v_exp_f32_e32 v46, v46\n\t"
  "v_exp_f32_e32 v47, v47\n\t"
  "s_cbranch_scc0 Lkp_%=\n\t"
  "s_setprio 0\n\t"
  "Lkp_%=:\n\t"
  "v_exp_f32_e32 v16, v16\n\t"
  "v_exp_f32_e32 v17, v17\n\t"
  "v_smfmac_f32_32x32x32_bf16 v[0:15], v[140:143], v[40:47], v164\n\t"
  "v_exp_f32_e32 v18, v18\n\t"
  "v_exp_f32_e32 v19, v19\n\t"
  "v_exp_f32_e32 v20, v20\n\t"
  "v_mfma_f32_32x32x16_bf16 v[32:47], v[52:55], v[144:147], 0\n\t"
  "v_exp_f32_e32 v21, v21\n\t"
  "v_exp_f32_e32 v22, v22\n\t"
  "v_exp_f32_e32 v23, v23\n\t"
  "v_exp_f32_e32 v24, v24\n\t"
  "v_exp_f32_e32 v25, v25\n\t"
  "v_smfmac_f32_32x32x32_bf16 v[148:163], v[80:83], v[16:23], v164\n\t"
  "v_exp_f32_e32 v26, v26\n\t"
  "v_exp_f32_e32 v27, v27\n\t"
  "v_exp_f32_e32 v28, v28\n\t"
  "v_xor_b32_e32 v14, 0x6000, v165\n\t"
  "ds_write_b128 v14, v[0:3]\n\t"
  "ds_write_b128 v14, v[4:7] offset:16\n\t"
  "ds_write_b32 v14, v8 offset:32\n\t"
  "ds_write_b32 v14, v165 offset:44\n\t"
  "v_exp_f32_e32 v29, v29\n\t"
  "v_exp_f32_e32 v30, v30\n\t"
  "v_exp_f32_e32 v31, v31\n\t"
  "v_exp_f32_e32 v32, v32\n\t"
  "v_exp_f32_e32 v33, v33\n\t"
  "v_smfmac_f32_32x32x32_bf16 v[148:163], v[84:87], v[24:31], v164\n\t"
  "v_exp_f32_e32 v34, v34\n\t"
  "v_exp_f32_e32 v35, v35\n\t"
  "v_exp_f32_e32 v36, v36\n\t"
  "v_mfma_f32_32x32x16_bf16 v[16:31], v[56:59], v[144:147], 0\n\t"
  "v_exp_f32_e32 v37, v37\n\t"
  "v_exp_f32_e32 v38, v38\n\t"
  "v_exp_f32_e32 v39, v39\n\t"
  "v_exp_f32_e32 v40, v40\n\t"
  "v_exp_f32_e32 v41, v41\n\t"
  "v_smfmac_f32_32x32x32_bf16 v[148:163], v[88:91], v[32:39], v164\n\t"
  "v_exp_f32_e32 v42, v42\n\t"
  "v_exp_f32_e32 v43, v43\n\t"
  "v_exp_f32_e32 v44, v44\n\t"
  "v_exp_f32_e32 v45, v45\n\t"
  "v_exp_f32_e32 v46, v46\n\t"
  "v_exp_f32_e32 v47, v47\n\t"
  "v_exp_f32_e32 v16, v16\n\t"
  "v_exp_f32_e32 v17, v17\n\t"
  "v_smfmac_f32_32x32x32_bf16 v[148:163], v[92:95], v[40:47], v164\n\t"
  "v_exp_f32_e32 v18, v18\n\t"
  "v_exp_f32_e32 v19, v19\n\t"
  "v_exp_f32_e32 v20, v20\n\t"
  "v_mfma_f32_32x32x16_bf16 v[32:47], v[60:63], v[144:147], 0\n\t"
  "v_exp_f32_e32 v21, v21\n\t"
  "v_exp_f32_e32 v22, v22\n\t"
  "v_exp_f32_e32 v23, v23\n\t"
  "v_exp_f32_e32 v24, v24\n\t"
  "v_exp_f32_e32 v25, v25\n\t"
  "v_smfmac_f32_32x32x32_bf16 v[148:163], v[96:99], v[16:23], v164\n\t"
  "v_exp_f32_e32 v26, v26\n\t"
  "v_exp_f32_e32 v27, v27\n\t"
  "v_exp_f32_e32 v28, v28\n\t"
  "v_exp_f32_e32 v29, v29\n\t"
  "v_exp_f32_e32 v30, v30\n\t"
  "v_exp_f32_e32 v31, v31\n\t"
  "v_exp_f32_e32 v32, v32\n\t"
  "v_exp_f32_e32 v33, v33\n\t"
  "v_smfmac_f32_32x32x32_bf16 v[148:163], v[100:103], v[24:31], v164\n\t"
  "v_exp_f32_e32 v34, v34\n\t"
  "v_exp_f32_e32 v35, v35\n\t"
  "v_exp_f32_e32 v36, v36\n\t"
  "v_mfma_f32_32x32x16_bf16 v[16:31], v[64:67], v[144:147], 0\n\t"
  "v_exp_f32_e32 v37, v37\n\t"
  "v_exp_f32_e32 v38, v38\n\t"
  "v_exp_f32_e32 v39, v39\n\t"
  "v_exp_f32_e32 v40, v40\n\t"
  "v_exp_f32_e32 v41, v41\n\t"
  "v_smfmac_f32_32x32x32_bf16 v[148:163], v[104:107], v[32:39], v164\n\t"
  "v_exp_f32_e32 v42, v42\n\t"
  "v_exp_f32_e32 v43, v43\n\t"
  "v_exp_f32_e32 v44, v44\n\t"
  "v_exp_f32_e32 v45, v45\n\t"
  "v_exp_f32_e32 v46, v46\n\t"
  "v_exp_f32_e32 v47, v47\n\t"
  "v_exp_f32_e32 v16, v16\n\t"
  "v_exp_f32_e32 v17, v17\n\t"
  "v_smfmac_f32_32x32x32_bf16 v[148:163], v[108:111], v[40:47], v164\n\t"
  "v_exp_f32_e32 v18, v18\n\t"
  "v_exp_f32_e32 v19, v19\n\t"
  "v_exp_f32_e32 v20, v20\n\t"
  "v_mfma_f32_32x32x16_bf16 v[32:47], v[68:71], v[144:147], 0\n\t"
  "v_exp_f32_e32 v21, v21\n\t"
  "v_exp_f32_e32 v22, v22\n\t"
  "v_exp_f32_e32 v23, v23\n\t"
  "v_exp_f32_e32 v24, v24\n\t"
  "v_exp_f32_e32 v25, v25\n\t"
  "v_smfmac_f32_32x32x32_bf16 v[148:163], v[112:115], v[16:23], v164\n\t"
  "v_exp_f32_e32 v26, v26\n\t"
  "v_exp_f32_e32 v27, v27\n\t"
  "v_exp_f32_e32 v28, v28\n\t"
  "v_exp_f32_e32 v29, v29\n\t"
  "v_exp_f32_e32 v30, v30\n\t"
  "v_exp_f32_e32 v31, v31\n\t"
  "v_exp_f32_e32 v32, v32\n\t"
  "v_exp_f32_e32 v33, v33\n\t"
  "v_smfmac_f32_32x32x32_bf16 v[148:163], v[116:119], v[24:31], v164\n\t"
  "v_exp_f32_e32 v34, v34\n\t"
  "v_exp_f32_e32 v35, v35\n\t"
  "v_exp_f32_e32 v36, v36\n\t"
  "v_mfma_f32_32x32x16_bf16 v[16:31], v[72:75], v[144:147], 0\n\t"
  "v_exp_f32_e32 v37, v37\n\t"
  "v_exp_f32_e32 v38, v38\n\t"
  "v_exp_f32_e32 v39, v39\n\t"
  "v_exp_f32_e32 v40, v40\n\t"
  "v_exp_f32_e32 v41, v41\n\t"
  "v_smfmac_f32_32x32x32_bf16 v[148:163], v[120:123], v[32:39], v164\n\t"
  "v_exp_f32_e32 v42, v42\n\t"
  "v_exp_f32_e32 v43, v43\n\t"
  "v_exp_f32_e32 v44, v44\n\t"
  "v_exp_f32_e32 v45, v45\n\t"
  "v_exp_f32_e32 v46, v46\n\t"
  "v_exp_f32_e32 v47, v47\n\t"
  "v_exp_f32_e32 v16, v16\n\t"
  "v_exp_f32_e32 v17, v17\n\t"
  "v_smfmac_f32_32x32x32_bf16 v[148:163], v[124:127], v[40:47], v164\n\t"
  "v_exp_f32_e32 v18, v18\n\t"
  "v_exp_f32_e32 v19, v19\n\t"
  "v_exp_f32_e32 v20, v20\n\t"
  "v_mfma_f32_32x32x16_bf16 v[32:47], v[76:79], v[144:147], 0\n\t"
  "v_exp_f32_e32 v21, v21\n\t"
  "v_exp_f32_e32 v22, v22\n\t"
  "v_exp_f32_e32 v23, v23\n\t"
  "v_exp_f32_e32 v24, v24\n\t"
  "v_exp_f32_e32 v25, v25\n\t"
  "v_smfmac_f32_32x32x32_bf16 v[148:163], v[128:131], v[16:23], v164\n\t"
  "v_exp_f32_e32 v26, v26\n\t"
  "v_exp_f32_e32 v27, v27\n\t"
  "v_exp_f32_e32 v28, v28\n\t"
  "v_exp_f32_e32 v29, v29\n\t"
  "v_exp_f32_e32 v30, v30\n\t"
  "v_exp_f32_e32 v31, v31\n\t"
  "v_exp_f32_e32 v32, v32\n\t"
  "v_exp_f32_e32 v33, v33\n\t"
  "v_smfmac_f32_32x32x32_bf16 v[148:163], v[132:135], v[24:31], v164\n\t"
  "v_exp_f32_e32 v34, v34\n\t"
  "v_exp_f32_e32 v35, v35\n\t"
  "v_exp_f32_e32 v36, v36\n\t"
  "v_exp_f32_e32 v37, v37\n\t"
  "v_exp_f32_e32 v38, v38\n\t"
  "v_exp_f32_e32 v39, v39\n\t"
  "v_exp_f32_e32 v40, v40\n\t"
  "v_exp_f32_e32 v41, v41\n\t"
  "v_smfmac_f32_32x32x32_bf16 v[148:163], v[136:139], v[32:39], v164\n\t"
  "v_exp_f32_e32 v42, v42\n\t"
  "v_exp_f32_e32 v43, v43\n\t"
  "v_exp_f32_e32 v44, v44\n\t"
  "v_exp_f32_e32 v45, v45\n\t"
  "v_exp_f32_e32 v46, v46\n\t"
  "v_exp_f32_e32 v47, v47\n\t"
  "s_nop 1\n\t"
  "v_smfmac_f32_32x32x32_bf16 v[148:163], v[140:143], v[40:47], v164\n\t"
  "s_nop 12\n\t"
  "v_xor_b32_e32 v162, 0x7000, v165\n\t"
  "ds_write_b128 v162, v[148:151]\n\t"
  "ds_write_b128 v162, v[152:155] offset:16\n\t"
  "ds_write_b32 v162, v156 offset:32\n\t"
  "ds_write_b32 v162, v165 offset:44\n\t"
  "s_branch Lend_%=\n\t"
  "Lqs_%=:\n\t"
  "s_sleep 2\n\t"
  "ds_read_b32 v154, v152\n\t"
  "s_waitcnt lgkmcnt(0)\n\t"
  "v_cmp_gt_u32_e32 vcc, 4, v154\n\t"
  "s_cbranch_vccnz Lqs_%=\n\t"
  "s_branch Lqr_%=\n\t"
  "Ls1_%=:\n\t"
  "s_sleep 2\n\t"
  "ds_read_b32 v15, v14 offset:36\n\t"
  "s_waitcnt lgkmcnt(0)\n\t"
  "v_cmp_eq_u32_e32 vcc, 0, v15\n\t"
  "s_cbranch_vccnz Ls1_%=\n\t"
  "s_branch Lr1_%=\n\t"
  "Ls2_%=:\n\t"
  "s_sleep 2\n\t"
  "ds_read_b32 v163, v162 offset:36\n\t"
  "s_waitcnt lgkmcnt(0)\n\t"
  "v_cmp_eq_u32_e32 vcc, 0, v163\n\t"
  "s_cbranch_vccnz Ls2_%=\n\t"
  "s_branch Lr2_%=\n\t"
  "Ls3_%=:\n\t"
  "s_sleep 2\n\t"
  "ds_read_b32 v15, v14 offset:40\n\t"
  "s_waitcnt lgkmcnt(0)\n\t"
  "v_cmp_eq_u32_e32 vcc, 0, v15\n\t"
  "s_cbranch_vccnz Ls3_%=\n\t"
  "s_branch Lr3_%=\n\t"
  "Ls4_%=:\n\t"
  "s_sleep 2\n\t"
  "ds_read_b32 v163, v162 offset:40\n\t"
  "s_waitcnt lgkmcnt(0)\n\t"
  "v_cmp_eq_u32_e32 vcc, 0, v163\n\t"
  "s_cbranch_vccnz Ls4_%=\n\t"
  "s_branch Lr4_%=\n\t"
  "Ls5_%=:\n\t"
  "s_sleep 2\n\t"
  "ds_read_b32 v15, v14 offset:44\n\t"
  "s_waitcnt lgkmcnt(0)\n\t"
  "v_cmp_eq_u32_e32 vcc, 0, v15\n\t"
  "s_cbranch_vccnz Ls5_%=\n\t"
  "s_branch Lr5_%=\n\t"
  "Ls6_%=:\n\t"
  "s_sleep 2\n\t"
  "ds_read_b32 v163, v162 offset:44\n\t"
  "s_waitcnt lgkmcnt(0)\n\t"
  "v_cmp_eq_u32_e32 vcc, 0, v163\n\t"
  "s_cbranch_vccnz Ls6_%=\n\t"
  "s_branch Lr6_%=\n\t"
  "Lend_%=:\n\t"
  "s_waitcnt lgkmcnt(0)\n\t"
    :
      "+{v160}"(gk), "+{v161}"(gv), "+{v162}"(ov)
    : "{v164}"(0xDDDDu), "{v165}"(AT_RING + w * 2 * AT_SLOT + lane * 48), "{v166}"(AT_RING + (w & 3) * 2 * AT_SLOT + 3072 + lane * 16),
      [kb] "s"(kb), [vb] "s"(vb), [qb] "s"(qp), [rb] "s"(w >> 2), [qc] "s"(C_Q)
    : "memory", "scc", "vcc", "s76", "s77",
      "v0","v1","v2","v3","v4","v5","v6","v7","v8","v9","v10","v11","v12","v13","v14","v15","v16","v17","v18","v19","v20","v21","v22","v23","v24","v25","v26","v27","v28","v29","v30","v31","v32","v33","v34","v35","v36","v37","v38","v39","v40","v41","v42","v43","v44","v45","v46","v47","v48","v49","v50","v51","v52","v53","v54","v55","v56","v57","v58","v59","v60","v61","v62","v63","v64","v65","v66","v67","v68","v69","v70","v71","v72","v73","v74","v75","v76","v77","v78","v79","v80","v81","v82","v83","v84","v85","v86","v87","v88","v89","v90","v91","v92","v93","v94","v95","v96","v97","v98","v99","v100","v101","v102","v103","v104","v105","v106","v107","v108","v109","v110","v111","v112","v113","v114","v115","v116","v117","v118","v119","v120","v121","v122","v123","v124","v125","v126","v127","v128","v129","v130","v131","v132","v133","v134","v135","v136","v137","v138","v139","v140","v141","v142","v143","v144","v145","v146","v147","v148","v149","v150","v151","v152","v153","v154","v155","v156","v157","v158","v159","v163");
}
DEV void attn_slow_tile(const Params& p, int head, int qtile, int lane, float* o) {
  u32x4 q; ld16_async(q, p.ws + WS_Q, ((head * 64 + qtile) * 64 + lane) * 16);
  const u32 ov = ((lane & 27) == 16) ? 0x3f803f80u : 0u;
  const u32 vo = ((lane & 15) + ((lane >> 5) << 4)) * 16;
  f32x16 O = {0}; float m = -3.0e38f;
  for (int t = 0; t < 64; t++) {
    u32x4 k, v0, v1;
    ld16_async(k, p.ws + WS_K, ((head * 64 + t) * 64 + lane) * 16);
    ld16_async(v0, p.ws + WS_V, (head * 64 + t) * 1024 + vo);
    ld16_async(v1, p.ws + WS_V, (head * 64 + t) * 1024 + 512 + vo);
    asm volatile("s_waitcnt vmcnt(0)" : "+v"(q), "+v"(k), "+v"(v0), "+v"(v1) :: "memory");
    if (lane & 16) { v0 = u32x4{ov, ov, ov, ov}; v1 = v0; }
    f32x16 z = {0};
    f32x16 s = mfma32(k, q, z);
    float tm = s[0];
    for (int i = 1; i < 16; i++) tm = fmaxf(tm, s[i]);
    tm = fmaxf(tm, xhalf(tm, lane));
    float mn = fmaxf(m, tm), al = __builtin_amdgcn_exp2f(m - mn);
    m = mn;
    for (int i = 0; i < 9; i++) O[i] *= al;
    for (int i = 0; i < 16; i++) s[i] = __builtin_amdgcn_exp2f(s[i] - m);
    typedef float f32x8 __attribute__((ext_vector_type(8)));
    typedef __bf16 bf16x16 __attribute__((ext_vector_type(16)));
    f32x8 lo = __builtin_shufflevector(s, s, 0, 1, 2, 3, 4, 5, 6, 7), hi = __builtin_shufflevector(s, s, 8, 9, 10, 11, 12, 13, 14, 15);
    O = __builtin_amdgcn_smfmac_f32_32x32x32_bf16(__builtin_bit_cast(bf16x8, v0), __builtin_bit_cast(bf16x16, lo), O, 0xDDDD, 0, 0);
    O = __builtin_amdgcn_smfmac_f32_32x32x32_bf16(__builtin_bit_cast(bf16x8, v1), __builtin_bit_cast(bf16x16, hi), O, 0xDDDD, 0, 0);
  }
  for (int i = 0; i < 9; i++) o[i] = O[i];
}
struct Side { u32x4 n, g0, g1; };
DEV u32 side_off(int head, int qb, int lane, int j) {
  if (j == 0) return WS_F1 + (lane < 32 ? (lane * 8 + part_group(head >> 2, 1)) * 128 + 96 : ((qb * 4 + (lane & 3)) * 8 + (head >> 2)) * 128 + 64) + ((head >> 1) & 1) * 16;
  return WS_F1 + (qb * 32 + (j - 1) * 16 + (lane >> 2)) * 128 + (lane & 3) * 16;
}
DEV void side_load(const Params& p, Side& s, int head, int qb, int lane) {
  ld16_async(s.n, p.ws, side_off(head, qb, lane, 0)); ld16_async(s.g0, p.ws, side_off(head, qb, lane, 1)); ld16_async(s.g1, p.ws, side_off(head, qb, lane, 2));
}
DEV void side_done(const Params& p, Side& s, int head, int qb, int lane, u64 tag) {
  if (__builtin_amdgcn_ballot_w64(unit_bad(s.n, tag) || unit_bad(s.g0, tag) || unit_bad(s.g1, tag)) != 0) {
    s.n = poll_flags(p.ws, side_off(head, qb, lane, 0), tag); s.g0 = poll_flags(p.ws, side_off(head, qb, lane, 1), tag); s.g1 = poll_flags(p.ws, side_off(head, qb, lane, 2), tag);
  }
  const float nm = row16_max(__uint_as_float((head & 1) ? s.n[2] : s.n[0]));
  const float bound2 = fmaxf(rdlane(nm, 0), rdlane(nm, 16)) * rdlane(nm, 32);
  *(LAS u32x2*)(size_t)(AT_GATE + lane * 8) = u32x2{s.g0[0], s.g0[2]};
  *(LAS u32x2*)(size_t)(AT_GATE + 512 + lane * 8) = u32x2{s.g1[0], s.g1[2]};
  if (lane == 0) *(volatile LAS u32*)(size_t)C_S = (bound2 <= SAFE_BOUND2) ? 1u : 2u;
}
DEV void attn_finalize(const Params& p, int head, int qb, int w, int lane, u64 tag) {
  const int qt = (7 - (w >> 2)) ^ (2 * (w & 3));
  const u32 a = AT_RING + qt * AT_SLOT + lane * 48;
  u32x2 vd; u32 gt; u32x4 a0, a1, a2, b0, b1, b2;
  for (;;) {
    asm volatile("ds_read_b128 %2, %8 offset:32\n\tds_read_b128 %5, %8 offset:32800\n\tds_read_b64 %6, %9\n\t"
                 "ds_read_b128 %0, %8\n\tds_read_b128 %1, %8 offset:16\n\tds_read_b128 %3, %8 offset:32768\n\tds_read_b128 %4, %8 offset:32784\n\t"
                 "ds_read_b32 %7, %10\n\ts_waitcnt lgkmcnt(0)"
                 : "=&v"(a0), "=&v"(a1), "=&v"(a2), "=&v"(b0), "=&v"(b1), "=&v"(b2), "=&v"(vd), "=&v"(gt)
                 : "v"(a), "v"(C_S), "v"(AT_GATE + (qt * 32 + (lane & 31)) * 4) : "memory");
    if (__builtin_amdgcn_ballot_w64(a2[3] <= 0xDDDDu || b2[3] <= 0xDDDDu || vd[0] == 0 || vd[1] < 4) == 0) break;
    __builtin_amdgcn_s_sleep(1);
  }
  const float gate = __uint_as_float(gt);
  float o[9];
  if (__builtin_amdgcn_readfirstlane(vd[0]) == 1) {
    for (int i = 0; i < 4; i++) { o[i] = __uint_as_float(a0[i]) + __uint_as_float(b0[i]); o[4 + i] = __uint_as_float(a1[i]) + __uint_as_float(b1[i]); }
    o[8] = __uint_as_float(a2[0]) + __uint_as_float(b2[0]);
  } else {
    attn_slow_tile(p, head, qb * 8 + qt, lane, o);
  }
  const float sc = gate * __builtin_amdgcn_rcpf(o[8]);
  u32x4 r = {min(pack2(o[0] * sc, o[1] * sc), EMPTY - 1), min(pack2(o[2] * sc, o[3] * sc), EMPTY - 1), min(pack2(o[4] * sc, o[5] * sc), EMPTY - 1), min(pack2(o[6] * sc, o[7] * sc), EMPTY - 1)};
  st16_wt(p.ws + WS_O, (((qb * 8 + qt) * 32 + head) * 64 + lane) * 16, r);
  garrive(C_O, lane);
}
DEV void o_clear(const Params& p, int head, int qb, int w, int lane) {
  const u32 o = (((qb * 8 + 2 * w) * 32 + head) * 64 + lane) * 16;
  st16_wt(p.ws + WS_O, o, u32x4{EMPTY, EMPTY, EMPTY, EMPTY}); st16_wt(p.ws + WS_O, o + 32768, u32x4{EMPTY, EMPTY, EMPTY, EMPTY});
}

template <int N> DEV void waitv(u32x4& a) { asm volatile("s_waitcnt vmcnt(%1)" : "+v"(a) : "n"(N) : "memory"); }
template <int N> DEV void waitv(u32x4& a, u32x4& b) { asm volatile("s_waitcnt vmcnt(%2)" : "+v"(a), "+v"(b) : "n"(N) : "memory"); }
template <int I, int N, class F> DEV void sfor(F&& f) { if constexpr (I < N) { f(std::integral_constant<int, I>{}); sfor<I + 1, N>(f); } }
DEV u32x4 pack8(const f32x16& a, int o) { return u32x4{pack2(a[o], a[o + 1]), pack2(a[o + 2], a[o + 3]), pack2(a[o + 4], a[o + 5]), pack2(a[o + 6], a[o + 7])}; }

DEV void gemm1(const Params& p, int dt, int ft, int ks, int w, int lane, u64 tag, u32 w2off, u32 wooff, int st, int head, int qb) {
  constexpr int HD = 8;
  u32 off[3];
  for (int j = 0; j < 3; j++) {
    int e = (w * 3 + j) * 64 + lane, r = e / 12, c = e - r * 12;
    if (ft == 14) c &= 3;
    off[j] = r * 2752 + c * 16;
  }
  const char* wb = (const char*)p.w1 + (size_t)ks * 512 * 2752 + ft * 192;
  auto stage = [&](int s) {
    #pragma unroll
    for (int j = 0; j < 3; j++) glds16(wb + s * 64 * 2752, off[j], LW + s * 12288 + (w * 3 + j) * 1024);
  };
  const int c31 = lane & 31, h = lane >> 5;
  const char* hb = p.ws + WS_H + (size_t)ks * 64 * 8192;
  const u32 hoff = h * 8192 + (dt * 128 + w * 32 + c31) * 16;
  u32 wa[2];
  { int g4 = (lane >> 4) & 1, i = lane & 15; wa[0] = LW + (8 * h + (i >> 2)) * 192 + g4 * 32 + (i & 3) * 8; wa[1] = wa[0] + 16 * 3072; }
  asm volatile("" : "+v"(wa[1]));
  gwait(C_P1, 8);
  stage(0); stage(1); stage(2);
  while (lds_poll(C_Q) < 4) __builtin_amdgcn_s_sleep(4);
  poll_flags(p.ws, WS_F1 + (ks * 64 + w * 16 + (lane & 15)) * 128, tag);
  garrive(C_G, lane); gwait(C_G, 4);
  u32x4 hf[HD];
  #pragma unroll
  for (int i = 0; i < HD; i++) ld16_async(hf[i], hb + i * 16384, hoff);
  f32x16 acc[3] = {};
  u32 pw2, pwo, pbb, pbc;
  Side sd;
  u32x4 wf[3][3];
  #pragma unroll
  for (int k = 0; k < 2; k++)
    #pragma unroll
    for (int j = 0; j < 3; j++) wf[k][j] = lds_tr2(wa[0] + k * 3072 + j * 64, wa[0] + k * 3072 + j * 64 + 768);
  sfor<0, 32>([&](auto kk) {
    constexpr int k = kk.value;
    constexpr int young = [] { int y = (k < HD) ? HD - 1 - k : 0;
      for (int j = (k >= HD ? k - HD : 0); j < k; j++) { if (j + HD < 32 && j != k - HD) y += 1; if (j + 3 < 8) y += 3; if (j == 2 || j == 31 - HD) y += 2; if (j == 16) y += 5; } return y; }();
    if constexpr (k == 9) { VMWAIT(4); garrive(C_G, lane); }
    waitv<young>(hf[k % HD]);
    if constexpr (k == 10) gwait(C_G, 8);
    if constexpr (k + 2 < 32)
      #pragma unroll
      for (int j = 0; j < 3; j++) { const u32 a = wa[(k + 2) >> 4] + ((k + 2) & 15) * 3072 + j * 64; wf[(k + 2) % 3][j] = lds_tr2(a, a + 768); }
    __builtin_amdgcn_sched_barrier(0);
    #pragma unroll
    for (int j = 0; j < 3; j++) acc[j] = mfma32(wf[k % 3][j], hf[k % HD], acc[j]);
    __builtin_amdgcn_sched_barrier(0);
    if constexpr (k + HD < 32) ld16_async(hf[k % HD], hb + (k + HD) * 16384, hoff);
    if constexpr (k + 3 < 8) stage(k + 3);
    if constexpr (k == 2) { warm(pbb, p.b1, ft * 192 + ((lane & 1) && ft < 14 ? 128 : 0)); warm(pbc, p.b2, st * 128); }
    if constexpr (k == 16) o_clear(p, head, qb, w, lane);
    if constexpr (k == 16) side_load(p, sd, head, qb, lane);
    if constexpr (k == 31 - HD) { warm(pw2, p.w2, w2off); warm(pwo, p.wo, wooff); }
  });
  garrive(C_G, lane);
  if (w == ks) {
    #pragma unroll
    for (int j = 0; j < 3; j++) {
      *(LAS u32x4*)(size_t)(SPARE + 2 * j * AT_SLOT + lane * 16) = pack8(acc[j], 0);
      *(LAS u32x4*)(size_t)(SPARE + (2 * j + 1) * AT_SLOT + lane * 16) = pack8(acc[j], 8);
    }
    LGKMWAIT(0);
  } else {
    char* pb = p.ws + WS_P + (size_t)(((dt * 15 + ft) * 4 + w) * 4 + ks) * 6144;
    #pragma unroll
    for (int j = 0; j < 3; j++)
      if (ft < 14 || j == 0) { st16_wt(pb, j * 2048 + lane * 16, pack8(acc[j], 0)); st16_wt(pb, j * 2048 + 1024 + lane * 16, pack8(acc[j], 8)); }
  }
  asm volatile("s_waitcnt vmcnt(0)" : "+v"(pw2), "+v"(pwo), "+v"(pbb), "+v"(pbc), "+v"(sd.n), "+v"(sd.g0), "+v"(sd.g1) :: "memory");
  if (w == ks) { if (lane == 0) *(volatile LAS u32*)(size_t)C_P = 1; }
  else if (lane < 8) set_line(p.ws, WS_FP + (((dt * 15 + ft) * 4 + w) * 4 + ks) * 128, tag, lane);
  if (w == 3) side_done(p, sd, head, qb, lane, tag);
}
typedef float f32x2 __attribute__((ext_vector_type(2)));
DEV f32x2 unpk(u32 v) { return f32x2{bf_lo(v), bf_hi(v)}; }
DEV u32 gelu2(f32x2 a) {
  constexpr float c0 = -2.0f * 0.7978845608028654f * 1.4426950408889634f, c1 = c0 * 0.044715f;
  f32x2 z = (a * a * c1 + c0) * a;
  f32x2 e = f32x2{__builtin_amdgcn_exp2f(z[0]), __builtin_amdgcn_exp2f(z[1])} + 1.0f;
  f32x2 r = a * f32x2{__builtin_amdgcn_rcpf(e[0]), __builtin_amdgcn_rcpf(e[1])};
  return pack2(r[0], r[1]);
}
template <bool FULL>
DEV void gemm1_reduce(const Params& p, int dt, int ft, int ks, int w, int lane) {
  const char* pb = p.ws + WS_P + (size_t)((dt * 15 + ft) * 4 + ks) * 4 * 6144;
  const int h = lane >> 5, f0 = ft * 96 + w * 16, p2 = 4 + (w >> 1), f2 = ft * 96 + p2 * 16 + (w & 1) * 8;
  u32x4 v[3]; u32x2 x[3], b0, b1, b2;
  #pragma unroll
  for (int s = 0; s < 3; s++) ld16_async(v[s], pb + (s + (s >= ks)) * 6144 + w * 1024, lane * 16);
  asm volatile("global_load_dwordx2 %0, %1, %2" : "=v"(b0) : "v"((f0 + 4 * h) * 2), "s"(p.b1) : "memory");
  asm volatile("global_load_dwordx2 %0, %1, %2 offset:16" : "=v"(b1) : "v"((f0 + 4 * h) * 2), "s"(p.b1) : "memory");
  const u32x4 own = lds_r128(SPARE + w * AT_SLOT + lane * 16);
  u32x2 own2;
  if (FULL) {
    #pragma unroll
    for (int s = 0; s < 3; s++)
      asm volatile("global_load_dwordx2 %0, %1, %2 sc1" : "=v"(x[s]) : "v"(lane * 16), "s"(pb + (s + (s >= ks)) * 6144 + p2 * 1024 + (w & 1) * 8) : "memory");
    asm volatile("global_load_dwordx2 %0, %1, %2" : "=v"(b2) : "v"((f2 + 4 * h) * 2), "s"(p.b1) : "memory");
    own2 = *(const LAS u32x2*)(size_t)(SPARE + p2 * AT_SLOT + (w & 1) * 8 + lane * 16);
  }
  const u32 uo = (h * 512 + dt * 128 + ks * 32 + (lane & 31)) * 16;
  asm volatile("s_waitcnt vmcnt(%5)" : "+v"(v[0]), "+v"(v[1]), "+v"(v[2]), "+v"(b0), "+v"(b1) : "n"(FULL ? 4 : 0) : "memory");
  u32x4 o;
  #pragma unroll
  for (int i = 0; i < 4; i++) o[i] = gelu2(((unpk(v[0][i]) + unpk(v[1][i])) + (unpk(v[2][i]) + unpk(own[i]))) + unpk(i < 2 ? b0[i] : b1[i - 2]));
  st16_wt(p.ws + WS_U + (size_t)(f0 >> 4) * 16384, uo, o);
  if (FULL) {
    asm volatile("s_waitcnt vmcnt(1)" : "+v"(x[0]), "+v"(x[1]), "+v"(x[2]), "+v"(b2) :: "memory");
    u32x2 o2;
    #pragma unroll
    for (int i = 0; i < 2; i++) o2[i] = gelu2(((unpk(x[0][i]) + unpk(x[1][i])) + (unpk(x[2][i]) + unpk(own2[i]))) + unpk(b2[i]));
    __builtin_amdgcn_raw_buffer_store_b64(o2, mkrsrc(p.ws + WS_U + (size_t)(ft * 6 + p2) * 16384 + (w & 1) * 8), uo, 0, 16);
  }
}

constexpr int G2_RING = 12;
struct G2 { u32 wv, ring; const char* w2b; int nk; };
DEV void g2_issue(const G2& c, int k) { u32 d = c.ring + (k % G2_RING) * 2048; const char* b = c.w2b + (size_t)k * 65536; glds16(b, c.wv, d); glds16(b + 32768, c.wv, d + 1024); }
DEV G2 g2_ctx(const Params& p, int st, int w, int lane) {
  G2 c;
  const int k0 = (w < 2) ? 22 * w : 44 + 21 * (w - 2);
  c.nk = (w < 2) ? 22 : 21;
  c.ring = LW + w * G2_RING * 2048;
  { int r = lane >> 3; c.wv = (((lane & 7) ^ (((r >> 1) & 1) << 2)) << 4) | (r << 12); }
  c.w2b = (const char*)p.w2 + (size_t)k0 * 65536 + st * 128;
  return c;
}
DEV void gemm2_prefetch(const Params& p, int st, int w, int lane) {
  const G2 c = g2_ctx(p, st, w, lane);
  #pragma unroll
  for (int k = 0; k < G2_RING; k++) g2_issue(c, k);
}
DEV bool any_empty(const u32x4& a, const u32x4& b) { return max(max(max(a[0], a[1]), max(a[2], a[3])), max(max(b[0], b[1]), max(b[2], b[3]))) == EMPTY; }
DEV void ld16_again(u32x4& d, const void* base, u32 voff) {
  asm volatile("global_load_dwordx4 %0, %1, %2 sc1" : "+v"(d) : "v"(voff), "s"(base) : "memory");
}
DEV void gemm2_out(const Params& p, int st, int dt, int w, int lane, u64 tag) {
  const G2 c = g2_ctx(p, st, w, lane);
  const int c31 = lane & 31, h = lane >> 5;
  const int k0 = (w < 2) ? 22 * w : 44 + 21 * (w - 2);
  const u32 ring = c.ring;
  const char* ub = p.ws + WS_U + (size_t)k0 * 16384;
  const int js = w & 1, jd = w >> 1;
  const u32 uoff = h * 8192 + (dt * 64 + jd * 32 + c31) * 16;
  const char* ub1 = ub + (jd ? -512 : 512);
  u32 tr;
  { int g4 = (lane >> 4) & 1, i = lane & 15, r = 4 * h + (i >> 2);
    tr = ring + r * 128 + (((2 * g4 + ((i & 3) >> 1)) ^ (((r >> 1) & 1) << 2)) << 4) + (i & 1) * 8; }
  f32x16 acc[2][2] = {};
  float s0, s1, b0;
  {
    const u32 ta0 = tr ^ (js * 64), ta1 = ta0 ^ 64;
    const u32 wv2 = (c.wv & 0xfff) | ((lane >> 3) << 10);
    const char* wob = (const char*)p.wo + (size_t)w * 128 * 1024 + dt * 128;
    constexpr int PD = 6;
    u32x4 uf[PD][2];
    u32 g0, g1, bs;
    const int hd = w * 8 + (lane & 7);
    const u32 fco = WS_FU + (((hd & 3) * 4 + (hd >> 3)) * 16 + ((hd >> 2) & 1) * 8 + (st >> 2)) * 128;
    u32x4 fc;
    ld4_async(g0, p.ws + WS_F1, (st * 8 + js * 4 + (c31 >> 3)) * 128 + (c31 & 7) * 8);
    ld4_async(g1, p.ws + WS_F1, (st * 8 + (js ^ 1) * 4 + (c31 >> 3)) * 128 + (c31 & 7) * 8);
    asm volatile("global_load_ushort %0, %1, %2" : "=v"(bs) : "v"((st * 64 + js * 32 + c31) * 2), "s"(p.b2) : "memory");
    #pragma unroll
    for (int k = 0; k < PD; k++) { ld16_async(uf[k][0], ub + k * 16384, uoff); ld16_async(uf[k][1], ub1 + k * 16384, uoff); }
    u32x4 bf[3][2];
    __builtin_amdgcn_s_setprio(1);
    bf[0][0] = lds_tr2(ta0, ta0 + 1024); bf[0][1] = lds_tr2(ta1, ta1 + 1024);
    bf[1][0] = lds_tr2(ta0 + 2048, ta0 + 3072); bf[1][1] = lds_tr2(ta1 + 2048, ta1 + 3072);
    sfor<0, 22>([&](auto kk) {
      constexpr int k = kk.value;
      if (k < 21 || c.nk == 22) {
        constexpr int yu = [] { int y = (k < PD) ? 2 * (PD - 1 - k) : 0;
          for (int j = (k >= PD ? k - PD : 0); j < k; j++) { if (j != k - PD && j + PD < 21) y += 2; if (j + G2_RING < 21) y += 2; if (j >= 12 && j < 20) y += 2; if (j == 12) y += 1; } return y; }();
        waitv<yu>(uf[k % PD][0], uf[k % PD][1]);
        if constexpr (k + 2 < 22) { constexpr u32 s = ((k + 2) % G2_RING) * 2048; bf[(k + 2) % 3][0] = lds_tr2(ta0 + s, ta0 + s + 1024); bf[(k + 2) % 3][1] = lds_tr2(ta1 + s, ta1 + s + 1024); }
        __builtin_amdgcn_sched_barrier(0);
        acc[0][0] = mfma32(uf[k % PD][0], bf[k % 3][0], acc[0][0]); acc[0][1] = mfma32(uf[k % PD][0], bf[k % 3][1], acc[0][1]);
        acc[1][0] = mfma32(uf[k % PD][1], bf[k % 3][0], acc[1][0]); acc[1][1] = mfma32(uf[k % PD][1], bf[k % 3][1], acc[1][1]);
        __builtin_amdgcn_sched_barrier(0);
        if (k + PD < 21 || (k + PD == 21 && c.nk == 22)) { ld16_async(uf[k % PD][0], ub + (k + PD) * 16384, uoff); ld16_async(uf[k % PD][1], ub1 + (k + PD) * 16384, uoff); }
        if (k + G2_RING < 21 || (k + G2_RING == 21 && c.nk == 22)) g2_issue(c, k + G2_RING);
        if constexpr (k == 12) ld16_async(fc, p.ws, fco);
        if constexpr (k >= 12 && k < 20) { glds16(wob + (k - 12) * 16384, wv2, ring + (k - 12) * 2048); glds16(wob + (k - 12) * 16384 + 8192, wv2, ring + (k - 12) * 2048 + 1024); }
      }
    });
    __builtin_amdgcn_s_setprio(3);
    asm volatile("s_waitcnt vmcnt(16)" : "+v"(g0), "+v"(g1), "+v"(bs), "+v"(fc) :: "memory");
    if (__builtin_amdgcn_ballot_w64(unit_bad(fc, tag)) != 0) poll_flags(p.ws, fco, tag);
    s0 = 1.0f - __uint_as_float(g0); s1 = 1.0f - __uint_as_float(g1); b0 = bf_lo(bs) * s0;
  }
  const bool early = lds_poll(C_O) < 8;
  auto scale = [&]() {
    for (int i = 0; i < 16; i++) { acc[0][0][i] = acc[0][0][i] * s0 + b0; acc[1][0][i] *= s0; acc[0][1][i] *= s1; acc[1][1][i] *= s1; }
    asm volatile("s_nop 1" : "+v"(acc[0][0]), "+v"(acc[0][1]), "+v"(acc[1][0]), "+v"(acc[1][1]));
  };
  if (early) { scale(); while (lds_poll(C_O) < 8) __builtin_amdgcn_s_sleep(2); }
  const char* ob0 = p.ws + WS_O + (size_t)((st * 2 + js) * 32 + w * 8) * 1024;
  const char* ob1 = p.ws + WS_O + (size_t)((st * 2 + (js ^ 1)) * 32 + w * 8) * 1024;
  u32x4 of[8][2];
  #pragma unroll
  for (int k = 0; k < 8; k++) {
    ld16_async(of[k][0], ob0 + k * 1024, lane * 16);
    ld16_async(of[k][1], ob1 + k * 1024, lane * 16);
  }
  if (!early) scale();
  VMWAIT(16);
  const u32 tm = tr ^ (jd * 64), to = tm ^ 64;
  u32x4 am[2], ao[2];
  am[0] = lds_tr2(tm, tm + 1024); ao[0] = lds_tr2(to, to + 1024);
  auto settle = [&](auto kk, u64 bad) {
    constexpr int k = kk.value;
    #pragma nounroll
    for (u32 spin = 0; spin < (1u << 18) && bad != 0; spin++) {
      __builtin_amdgcn_s_sleep(4);
      sfor<k, 8>([&](auto jj) { constexpr int j = jj.value; ld16_again(of[j][0], ob0 + j * 1024, lane * 16); ld16_again(of[j][1], ob1 + j * 1024, lane * 16); });
      waitv<14 - 2 * k>(of[k][0], of[k][1]);
      bad = __builtin_amdgcn_ballot_w64(any_empty(of[k][0], of[k][1]));
    }
  };
  waitv<14>(of[0][0], of[0][1]);
  settle(std::integral_constant<int, 0>{}, __builtin_amdgcn_ballot_w64(any_empty(of[0][0], of[0][1])));
  sfor<0, 8>([&](auto kk) {
    constexpr int k = kk.value;
    if constexpr (k + 1 < 8) { constexpr u32 o = (k + 1) * 2048; am[(k + 1) & 1] = lds_tr2(tm + o, tm + o + 1024); ao[(k + 1) & 1] = lds_tr2(to + o, to + o + 1024); }
    __builtin_amdgcn_sched_barrier(0);
    mfma32v(acc[1][1], ao[k & 1], of[k][1]); mfma32v(acc[1][0], ao[k & 1], of[k][0]);
    __builtin_amdgcn_sched_barrier(0);
    u64 bad = 0;
    if constexpr (k + 1 < 8) { waitv<12 - 2 * k>(of[k + 1][0], of[k + 1][1]); bad = __builtin_amdgcn_ballot_w64(any_empty(of[k + 1][0], of[k + 1][1])); }
    __builtin_amdgcn_sched_barrier(0);
    mfma32v(acc[0][1], am[k & 1], of[k][1]); mfma32v(acc[0][0], am[k & 1], of[k][0]);
    __builtin_amdgcn_sched_barrier(0);
    if constexpr (k + 1 < 8) settle(std::integral_constant<int, k + 1>{}, bad);
  });
  asm volatile("s_nop 8" : "+v"(acc[1][1]), "+v"(acc[1][0]));
  #pragma unroll
  for (int q = 3; q >= 1; q--) {
    if (q == 1) asm volatile("s_nop 8" : "+v"(acc[0][0]), "+v"(acc[0][1]));
    #pragma unroll
    for (int i = 0; i < 4; i++) {
      const f32x16& a = acc[q >> 1][q & 1];
      *(LAS f32x4*)(size_t)(ring + q * 4096 + i * 1024 + lane * 16) = f32x4{a[4 * i], a[4 * i + 1], a[4 * i + 2], a[4 * i + 3]};
    }
  }
  asm volatile("" ::: "memory");
  garrive(C_X, lane);
  f32x4 r[12];
  {
    const u32 a1 = LW + (w ^ 1) * G2_RING * 2048 + 4096 + lane * 16, a2 = LW + (w ^ 2) * G2_RING * 2048 + 8192 + lane * 16, a3 = LW + (w ^ 3) * G2_RING * 2048 + 12288 + lane * 16;
    u32 n;
    do {
      asm volatile("ds_read_b32 %12, %16\n\t"
                   "ds_read_b128 %0, %13\n\tds_read_b128 %1, %14\n\tds_read_b128 %2, %15\n\t"
                   "ds_read_b128 %3, %13 offset:1024\n\tds_read_b128 %4, %14 offset:1024\n\tds_read_b128 %5, %15 offset:1024\n\t"
                   "ds_read_b128 %6, %13 offset:2048\n\tds_read_b128 %7, %14 offset:2048\n\tds_read_b128 %8, %15 offset:2048\n\t"
                   "ds_read_b128 %9, %13 offset:3072\n\tds_read_b128 %10, %14 offset:3072\n\tds_read_b128 %11, %15 offset:3072\n\t"
                   "s_waitcnt lgkmcnt(0)"
                   : "=&v"(r[0]), "=&v"(r[1]), "=&v"(r[2]), "=&v"(r[3]), "=&v"(r[4]), "=&v"(r[5]), "=&v"(r[6]), "=&v"(r[7]), "=&v"(r[8]), "=&v"(r[9]), "=&v"(r[10]), "=&v"(r[11]), "=&v"(n)
                   : "v"(a1), "v"(a2), "v"(a3), "v"(C_X) : "memory");
    } while (__builtin_amdgcn_readfirstlane(n) < 4);
  }
  u32 pk[8];
  #pragma unroll
  for (int i = 0; i < 4; i++) {
    const f32x4 t = (r[3 * i] + r[3 * i + 1]) + r[3 * i + 2];
    pk[2 * i] = pack2(acc[0][0][4 * i] + t[0], acc[0][0][4 * i + 1] + t[1]); pk[2 * i + 1] = pack2(acc[0][0][4 * i + 2] + t[2], acc[0][0][4 * i + 3] + t[3]);
  }
  const u32 dst = ((st * 64 + js * 32 + c31) * 512 + dt * 64 + jd * 32 + h * 8) * 2;
  #pragma unroll
  for (int i = 0; i < 4; i += 2) {
    auto x = __builtin_amdgcn_permlane32_swap(pk[2 * i], pk[2 * i + 2], false, false);
    auto y = __builtin_amdgcn_permlane32_swap(pk[2 * i + 1], pk[2 * i + 3], false, false);
    __builtin_amdgcn_raw_buffer_store_b128(u32x4{x[0], y[0], x[1], y[1]}, __builtin_amdgcn_make_buffer_rsrc(p.out, 0, S_ * D_ * 2, 0x00020000), dst + i * 16, 0, 0);
  }
}

constexpr int CODE_LINES = 9;
__global__ void __launch_bounds__(768, 3) k_fused(Params p) {
  const int w = __builtin_amdgcn_readfirstlane(threadIdx.x >> 6), lane = threadIdx.x & 63;
  const int xcd = blockIdx.x & 7, bi = blockIdx.x >> 3;
  u32 cpf;
  {
    u64 pc; asm volatile("s_getpc_b64 %0" : "=s"(pc));
    const u32 o = (w == 8) ? (((lane < CODE_LINES ? lane : 0) * 32 + bi) * 128) : 0;
    asm volatile("global_load_dword %0, %1, %2" : "=v"(cpf) : "v"(o), "s"(pc & ~127ull) : "memory");
  }
  if (threadIdx.x < 32) *(LAS u32*)(size_t)(C_Q + threadIdx.x * 4) = 0;
  const int tg = (xcd >> 1) * 8 + (bi >> 2), g1 = (xcd & 1) * 4 + (bi & 3);
  if (w < 8) { glds16(p.x, (tg * 64 + g1 * 8 + w) * 1024 + lane * 16, P1_LN + w * 1024); glds16(p.gw, lane * 16, P1_LN + 8192 + w * 1024); }
  const int ks = xcd >> 1, dt1 = bi >> 3, ft = (xcd & 1) * 8 + (bi & 7);
  if (w < 8) {
    const u64 tag = (w < 4) ? p1_compute<false>(p, tg, g1, w, lane, cpf) : p1_compute<true>(p, tg, g1, w, lane, cpf);
    const int head = xcd * 4 + (bi >> 3), qb = bi & 7;
    __builtin_amdgcn_s_sleep(24);
    { const int pt = (lane >> 2) % 3;
      poll_flags(p.ws, WS_F1 + ((((lane < 12 && pt) ? w * 4 : qb * 4) + (lane & 3)) * 8 + part_group(xcd, lane < 12 ? pt : 0)) * 128, tag); }
    attn_wave(p, head, qb, w, lane);
    attn_finalize(p, head, qb, w, fresh_lane(), tag);
    __builtin_amdgcn_endpgm();
  } else {
    const int gw = w - 8;
    const int st = xcd * 4 + (bi >> 3), dt2 = bi & 7;
    const bool has1 = ft < 15;
    const u32 w1off = (ks * 512 + bi * 16 + gw * 4 + (lane >> 4)) * 2752 + (xcd & 1) * 1536 + ((lane & 15) < ((xcd & 1) ? 10 : 13) ? (lane & 15) * 128 : 0);
    u32 pw = 0;
    const u64 tag = p1_loader(p, tg, g1, w, lane, w1off, pw);
    asm volatile("s_waitcnt vmcnt(0)" : "+v"(pw), "+v"(cpf) :: "memory");
    __builtin_amdgcn_s_setprio(3);
    const u32 w2off = (dt2 * 172 + gw * 43 + (lane < 43 ? lane : 0)) * 4096 + st * 128, wooff = ((st & 3) * 128 + gw * 32 + (lane & 31)) * 1024 + dt2 * 128;
    if (has1) {
      gemm1(p, dt1, ft, ks, gw, lane, tag, w2off, wooff, st, xcd * 4 + (bi >> 3), bi & 7);
      gwait(C_G, 12);
      gemm2_prefetch(p, st, gw, lane);
      if (ft < 14 || gw < 2) {
        { const int sq = (lane & 3) == ks ? (ks ^ 1) : (lane & 3); poll_flags(p.ws, WS_FP + (((dt1 * 15 + ft) * 4 + ks) * 4 + sq) * 128, tag); }
        gwait(C_P, 1);
        if (ft < 14) gemm1_reduce<true>(p, dt1, ft, ks, gw, lane); else gemm1_reduce<false>(p, dt1, ft, ks, gw, lane);
        VMWAIT(0);
      }
      if (arrive_last(C_U, 4, lane) && lane < 8) set_line(p.ws, WS_FU + ((dt1 * 4 + ks) * 16 + ft) * 128, tag, lane);
    } else {
      u32 pb, pc;
      while (lds_poll(C_KV) < 8) __builtin_amdgcn_s_sleep(4);
      if (gw == 3) {
        Side sd;
        side_load(p, sd, xcd * 4 + (bi >> 3), bi & 7, lane);
        asm volatile("s_waitcnt vmcnt(0)" : "+v"(sd.n), "+v"(sd.g0), "+v"(sd.g1) :: "memory");
        side_done(p, sd, xcd * 4 + (bi >> 3), bi & 7, lane, tag);
      }
      o_clear(p, xcd * 4 + (bi >> 3), bi & 7, gw, lane);
      __builtin_amdgcn_s_sleep(64);
      warm(pb, p.w2, w2off); warm(pc, p.wo, wooff);
      asm volatile("s_waitcnt vmcnt(0)" : "+v"(pb), "+v"(pc) :: "memory");
      if (arrive_last(C_U, 4, lane) && lane < 8) set_line(p.ws, WS_FU + ((dt1 * 4 + ks) * 16 + ft) * 128, tag, lane);
      gemm2_prefetch(p, st, gw, lane);
    }
    {
      const int k0 = (gw < 2) ? 22 * gw : 44 + 21 * (gw - 2), t0 = k0 / 6, t1 = (k0 + (gw < 2 ? 21 : 20)) / 6;
      int l = lane & 31, fti = t0 + (l >> 1), ds = dt2 * 2 + (l & 1);
      if (fti > t1) fti = t1;
      poll_flags(p.ws, lane < 32 ? WS_FU + (ds * 16 + fti) * 128 : WS_F1 + (st * 8 + (l >> 2)) * 128 + (l & 3) * 16, tag);
    }
    gemm2_out(p, st, dt2, gw, lane, tag);
    if (blockIdx.x == 0 && gw == 0 && lane == 0) __builtin_amdgcn_raw_buffer_store_b64(u32x2{(u32)tag, (u32)(tag >> 32)}, mkrsrc(p.ws), WS_EPOCH, 0, 16);
    __builtin_amdgcn_endpgm();
  }
}

extern "C" void kernel_launch(void* const* d_in, const int* in_sizes, int n_in, void* d_out, int out_size, void* d_ws, size_t ws_size, hipStream_t stream) {
  Params p;
  p.x = (const u16*)d_in[0]; p.wqkv = (const u16*)d_in[1]; p.wo = (const u16*)d_in[2];
  p.w1 = (const u16*)d_in[4]; p.b1 = (const u16*)d_in[5]; p.w2 = (const u16*)d_in[6]; p.b2 = (const u16*)d_in[7]; p.gw = (const u16*)d_in[8];
  p.out = (u16*)d_out; p.ws = (char*)d_ws;
  (void)hipFuncSetAttribute((const void*)k_fused, hipFuncAttributeMaxDynamicSharedMemorySize, LDS_BYTES);
  k_fused<<<256, 768, LDS_BYTES, stream>>>(p);
}
```
